```python
import torch

def _softmax(x, axis=-1):
    return torch.softmax(x, dim=axis)
def _gelu(x):
    return torch.nn.functional.gelu(x, approximate="tanh")
def _silu(x):
    return torch.nn.functional.silu(x)
def _sigmoid(x):
    return torch.sigmoid(x)
def _layernorm(x, axis=-1, eps=1e-5):
    m = x.mean(dim=axis, keepdim=True)
    v = x.var(dim=axis, keepdim=True, unbiased=False)
    return (x - m) / torch.sqrt(v + eps)
def _scatter_add(tgt, idx, src, axis=0):
    return tgt.clone().index_add_(axis, idx, src)
def _segment_sum(src, seg, n):
    out = torch.zeros((n,) + src.shape[1:], dtype=src.dtype)
    return out.index_add_(0, seg, src)
def _pad(x, pw):
    flat = tuple(v for pair in reversed(pw) for v in pair)
    return torch.nn.functional.pad(x, flat)

kernel_name = 'standalone__gqa_softmax__near_ridge'

def setup_inputs():

    _w = (lambda a: (lambda t: t.to(torch.bfloat16) if t.is_floating_point() else t)(torch.as_tensor(a)))
    B, Hq, Hkv, Sq, Sk, D = 8, 32, 4, 48, 2048, 128
    return {
        "q": _w((lambda *s: torch.randn(*s, dtype=torch.bfloat16) * 0.8)(B, Hq, Sq, D)),
        "k": _w((lambda *s: torch.randn(*s, dtype=torch.bfloat16) * 0.8)(B, Hkv, Sk, D)),
        "v": _w((lambda *s: torch.randn(*s, dtype=torch.bfloat16) * 0.05)(B, Hkv, Sk, D)),
    }

def reference(q, k, v):
    B, Hq, Sq, D = q.shape
    Hkv = k.shape[1]
    rep = Hq // Hkv
    k = (lambda x, n, axis: torch.repeat_interleave(x, n, dim=axis))(k, rep, axis=1)
    v = (lambda x, n, axis: torch.repeat_interleave(x, n, dim=axis))(v, rep, axis=1)
    scale = D ** -0.5
    scores = torch.einsum("bhqd,bhkd->bhqk", q, k) * scale
    p = _softmax(scores, axis=-1)
    return torch.einsum("bhqk,bhkd->bhqd", p, v)
```

```cpp
#include <hip/hip_runtime.h>
#include <hip/hip_bf16.h>

typedef __bf16 bf16x8 __attribute__((ext_vector_type(8)));
typedef __bf16 bf16x2 __attribute__((ext_vector_type(2)));
typedef short s16x4 __attribute__((ext_vector_type(4)));
typedef float f32x4 __attribute__((ext_vector_type(4)));
typedef float f32x2 __attribute__((ext_vector_type(2)));
typedef unsigned u32x4 __attribute__((ext_vector_type(4)));
typedef unsigned u32x2 __attribute__((ext_vector_type(2)));

constexpr int B_ = 8, HQ = 32, HKV = 4, SQ = 48, SK = 2048, D = 128;
constexpr int NW = 8;
constexpr int KT = 32;
constexpr int KPW = SK / NW;
constexpr int NT = KPW / KT;
constexpr float SC = 0.088388347648318447f * 1.4426950408889634f;
constexpr float THR_RAW = 8.0f / SC;

constexpr int RING = 16384;
constexpr int PROW = 132;
constexpr int MERGE_BYTES = NW * 32 * PROW * 4;
constexpr int STAT_OFF = MERGE_BYTES;
constexpr int SMEM_BYTES = STAT_OFF + NW * SQ * 8;

#define DI __device__ __forceinline__

DI float max3f(float a, float b, float c) { float r; asm("v_max3_f32 %0, %1, %2, %3" : "=v"(r) : "v"(a), "v"(b), "v"(c)); return r; }
DI float max2f(float a, float b) { float r; asm("v_max_f32_e32 %0, %1, %2" : "=v"(r) : "v"(a), "v"(b)); return r; }
DI unsigned cvtpk(float lo, float hi) { f32x2 v = {lo, hi}; bf16x2 b = __builtin_convertvector(v, bf16x2); return __builtin_bit_cast(unsigned, b); }

DI void dma_k(unsigned o0, unsigned o1, unsigned o2, unsigned o3, const char* gb, unsigned ldsb) {
  unsigned keep;
  asm volatile("s_mov_b32 %0, m0\n\ts_mov_b32 m0, %6\n\ts_nop 0\n\t"
               "global_load_lds_dwordx4 %1, %5 offset:-4096\n\t"
               "global_load_lds_dwordx4 %2, %5 offset:-3072\n\t"
               "global_load_lds_dwordx4 %3, %5 offset:-2048\n\t"
               "global_load_lds_dwordx4 %4, %5 offset:-1024\n\t"
               "global_load_lds_dwordx4 %1, %5 offset:0\n\t"
               "global_load_lds_dwordx4 %2, %5 offset:1024\n\t"
               "global_load_lds_dwordx4 %3, %5 offset:2048\n\t"
               "global_load_lds_dwordx4 %4, %5 offset:3072\n\t"
               "s_mov_b32 m0, %0"
               : "=&s"(keep) : "v"(o0), "v"(o1), "v"(o2), "v"(o3), "s"(gb), "s"(ldsb) : "memory");
}
DI void dma_v(unsigned o0, unsigned o1, const char* gb, unsigned ldsb) {
  unsigned keep;
  asm volatile("s_mov_b32 %0, m0\n\ts_mov_b32 m0, %4\n\ts_nop 0\n\t"
               "global_load_lds_dwordx4 %1, %3 offset:-4096\n\t"
               "global_load_lds_dwordx4 %2, %3 offset:-3072\n\t"
               "global_load_lds_dwordx4 %1, %3 offset:-2048\n\t"
               "global_load_lds_dwordx4 %2, %3 offset:-1024\n\t"
               "global_load_lds_dwordx4 %1, %3 offset:0\n\t"
               "global_load_lds_dwordx4 %2, %3 offset:1024\n\t"
               "global_load_lds_dwordx4 %1, %3 offset:2048\n\t"
               "global_load_lds_dwordx4 %2, %3 offset:3072\n\t"
               "s_mov_b32 m0, %0"
               : "=&s"(keep) : "v"(o0), "v"(o1), "s"(gb), "s"(ldsb) : "memory");
}

typedef __attribute__((address_space(3))) char lds_char;
typedef __attribute__((address_space(3))) s16x4 lds_s16x4;
typedef __attribute__((address_space(3))) bf16x8 lds_bf16x8;

__global__ __launch_bounds__(64 * NW, 2)
void gqa_attn_kernel(const __hip_bfloat16* __restrict__ Q, const __hip_bfloat16* __restrict__ K,
                     const __hip_bfloat16* __restrict__ V, __hip_bfloat16* __restrict__ O) {
  __shared__ __attribute__((aligned(16))) char smem[SMEM_BYTES];
  const int tid = threadIdx.x, lane = tid & 63;
  const int wid = __builtin_amdgcn_readfirstlane(tid >> 6);
  const int bid = blockIdx.x;
  const int idx = bid >> 3;
  const int kvg = (bid & 7) * 4 + (idx >> 3);
  const int hq = kvg * 8 + (idx & 7);
  const int l15 = lane & 15, g = lane >> 4;

  const char* Kw = (const char*)K + ((size_t)kvg * SK + (size_t)wid * KPW) * (D * 2) + 4096;
  const char* Vw = (const char*)V + ((size_t)kvg * SK + (size_t)wid * KPW) * (D * 2) + 4096;
  lds_char* sm = (lds_char*)smem;
  const unsigned sbase = (unsigned)(size_t)sm;
  const unsigned kslot = sbase + wid * RING, vslot = kslot + 8192;

  const unsigned ko0 = (unsigned)(g * 256 + 16 * (l15 ^ g));
  const unsigned ko1 = ko0 ^ 64u, ko2 = ko0 ^ 128u, ko3 = ko0 ^ 192u;
  const unsigned vo0 = (unsigned)(g * 256 + 16 * (l15 ^ (g << 1)));
  const unsigned vo1 = vo0 ^ 128u;

  dma_k(ko0, ko1, ko2, ko3, Kw, kslot + 4096);
  dma_v(vo0, vo1, Vw, vslot + 4096);

  bf16x8 qf[3][4];
  {
    const char* Qp = (const char*)Q + ((size_t)hq * SQ + l15) * (D * 2) + g * 16;
#pragma unroll
    for (int qt = 0; qt < 3; ++qt)
#pragma unroll
      for (int s = 0; s < 4; ++s) qf[qt][s] = *(const bf16x8*)(Qp + qt * 16 * 256 + s * 64);
  }

  const unsigned ka0 = wid * RING + l15 * 256 + 16 * (g ^ l15);
  const int q4 = l15 >> 2, p4 = l15 & 3;
  const unsigned va0 = wid * RING + 8192 + (4 * g + q4) * 256 + 32 * (4 * (g & 1) + q4) + 16 * (p4 >> 1) + 8 * (p4 & 1);

  f32x4 o[8][3];
#pragma unroll
  for (int dt = 0; dt < 8; ++dt)
#pragma unroll
    for (int qt = 0; qt < 3; ++qt) o[dt][qt] = f32x4{0.f, 0.f, 0.f, 0.f};
  float m[3], l[3];
#pragma unroll
  for (int qt = 0; qt < 3; ++qt) { m[qt] = -__builtin_inff(); l[qt] = 0.f; }

  for (int t = 0; t < NT; ++t) {
    asm volatile("s_waitcnt vmcnt(8)" ::: "memory");
    bf16x8 kf[2][4];
#pragma unroll
    for (int kt = 0; kt < 2; ++kt)
#pragma unroll
      for (int s = 0; s < 4; ++s) kf[kt][s] = *(lds_bf16x8*)(sm + ((ka0 ^ (64u * s)) + 4096u * kt));
    asm volatile("s_waitcnt lgkmcnt(0)" ::: "memory");
    if (t + 1 < NT) dma_k(ko0, ko1, ko2, ko3, Kw + (size_t)(t + 1) * 8192, kslot + 4096);

    f32x4 s_[2][3];
#pragma unroll
    for (int kt = 0; kt < 2; ++kt)
#pragma unroll
      for (int qt = 0; qt < 3; ++qt) {
        f32x4 a = {0.f, 0.f, 0.f, 0.f};
#pragma unroll
        for (int s = 0; s < 4; ++s) a = __builtin_amdgcn_mfma_f32_16x16x32_bf16(kf[kt][s], qf[qt][s], a, 0, 0, 0);
        s_[kt][qt] = a;
      }

    if (t + 1 < NT) asm volatile("s_waitcnt vmcnt(8)" ::: "memory");
    else asm volatile("s_waitcnt vmcnt(0)" ::: "memory");
    s16x4 vlo[8], vhi[8];
#pragma unroll
    for (int dt = 0; dt < 8; ++dt) {
      vlo[dt] = __builtin_amdgcn_ds_read_tr16_b64_v4i16((lds_s16x4*)(sm + (va0 ^ (32u * dt))));
      vhi[dt] = __builtin_amdgcn_ds_read_tr16_b64_v4i16((lds_s16x4*)(sm + ((va0 ^ (32u * dt)) + 4096u)));
    }

    float lm[3];
    bool need = false;
#pragma unroll
    for (int qt = 0; qt < 3; ++qt) {
      float a = max3f(s_[0][qt][0], s_[0][qt][1], s_[0][qt][2]);
      a = max3f(a, s_[0][qt][3], s_[1][qt][0]);
      a = max3f(a, s_[1][qt][1], s_[1][qt][2]);
      a = max2f(a, s_[1][qt][3]);
      lm[qt] = a;
      need |= !(a <= m[qt] + THR_RAW);
    }
    if (__any(need)) {
#pragma unroll
      for (int qt = 0; qt < 3; ++qt) {
        float rm = lm[qt];
        auto r = __builtin_amdgcn_permlane16_swap(__float_as_uint(rm), __float_as_uint(rm), false, false);
        rm = fmaxf(__uint_as_float(r[0]), __uint_as_float(r[1]));
        auto r2 = __builtin_amdgcn_permlane32_swap(__float_as_uint(rm), __float_as_uint(rm), false, false);
        rm = fmaxf(__uint_as_float(r2[0]), __uint_as_float(r2[1]));
        const float mn = fmaxf(m[qt], rm);
        const float alpha = __builtin_amdgcn_exp2f((m[qt] - mn) * SC);
        m[qt] = mn;
        l[qt] *= alpha;
#pragma unroll
        for (int dt = 0; dt < 8; ++dt) o[dt][qt] *= alpha;
      }
    }
    bf16x8 pf[3];
#pragma unroll
    for (int qt = 0; qt < 3; ++qt) {
      const float nm = -m[qt] * SC;
      float p[8];
#pragma unroll
      for (int r = 0; r < 4; ++r) {
        p[r] = __builtin_amdgcn_exp2f(fmaf(s_[0][qt][r], SC, nm));
        p[4 + r] = __builtin_amdgcn_exp2f(fmaf(s_[1][qt][r], SC, nm));
      }
      l[qt] += ((p[0] + p[1]) + (p[2] + p[3])) + ((p[4] + p[5]) + (p[6] + p[7]));
      u32x4 w = {cvtpk(p[0], p[1]), cvtpk(p[2], p[3]), cvtpk(p[4], p[5]), cvtpk(p[6], p[7])};
      pf[qt] = __builtin_bit_cast(bf16x8, w);
    }

    asm volatile("s_waitcnt lgkmcnt(0)" ::: "memory");
    if (t + 1 < NT) dma_v(vo0, vo1, Vw + (size_t)(t + 1) * 8192, vslot + 4096);

#pragma unroll
    for (int dt = 0; dt < 8; ++dt) {
      typedef short s16x8 __attribute__((ext_vector_type(8)));
      s16x8 vv = {vlo[dt][0], vlo[dt][1], vlo[dt][2], vlo[dt][3], vhi[dt][0], vhi[dt][1], vhi[dt][2], vhi[dt][3]};
      const bf16x8 vf = __builtin_bit_cast(bf16x8, vv);
#pragma unroll
      for (int qt = 0; qt < 3; ++qt) o[dt][qt] = __builtin_amdgcn_mfma_f32_16x16x32_bf16(vf, pf[qt], o[dt][qt], 0, 0, 0);
    }
  }

  float* stat = (float*)(smem + STAT_OFF);
#pragma unroll
  for (int qt = 0; qt < 3; ++qt) {
    float x = l[qt];
    auto r = __builtin_amdgcn_permlane16_swap(__float_as_uint(x), __float_as_uint(x), false, false);
    x = __uint_as_float(r[0]) + __uint_as_float(r[1]);
    auto r2 = __builtin_amdgcn_permlane32_swap(__float_as_uint(x), __float_as_uint(x), false, false);
    x = __uint_as_float(r2[0]) + __uint_as_float(r2[1]);
    if (g == 0) *(f32x2*)(stat + (wid * SQ + qt * 16 + l15) * 2) = f32x2{m[qt], x};
  }
  __syncthreads();
  float f[3];
#pragma unroll
  for (int qt = 0; qt < 3; ++qt) {
    f32x2 ml[NW];
    float M = -__builtin_inff();
#pragma unroll
    for (int w = 0; w < NW; ++w) { ml[w] = *(const f32x2*)(stat + (w * SQ + qt * 16 + l15) * 2); M = fmaxf(M, ml[w][0]); }
    float L = 0.f;
#pragma unroll
    for (int w = 0; w < NW; ++w) L += ml[w][1] * __builtin_amdgcn_exp2f((ml[w][0] - M) * SC);
    f[qt] = __builtin_amdgcn_exp2f((m[qt] - M) * SC) / L;
  }
  float* mb = (float*)smem;
  __hip_bfloat16* Oh = O + (size_t)hq * SQ * D;
#pragma unroll
  for (int qt = 0; qt < 2; ++qt)
#pragma unroll
    for (int dt = 0; dt < 8; ++dt)
      *(f32x4*)(mb + (wid * 32 + qt * 16 + l15) * PROW + dt * 16 + g * 4) = o[dt][qt] * f[qt];
  __syncthreads();
#pragma unroll
  for (int i = 0; i < 2; ++i) {
    const int c = tid + i * 512, q = c >> 5, d4 = (c & 31) * 4;
    f32x4 a = *(const f32x4*)(mb + q * PROW + d4);
#pragma unroll
    for (int w = 1; w < NW; ++w) a += *(const f32x4*)(mb + (w * 32 + q) * PROW + d4);
    u32x2 pk = {cvtpk(a[0], a[1]), cvtpk(a[2], a[3])};
    *(u32x2*)(Oh + q * D + d4) = pk;
  }
  __syncthreads();
#pragma unroll
  for (int dt = 0; dt < 8; ++dt)
    *(f32x4*)(mb + (wid * 32 + l15) * PROW + dt * 16 + g * 4) = o[dt][2] * f[2];
  __syncthreads();
  {
    const int q = tid >> 5, d4 = (tid & 31) * 4;
    f32x4 a = *(const f32x4*)(mb + q * PROW + d4);
#pragma unroll
    for (int w = 1; w < NW; ++w) a += *(const f32x4*)(mb + (w * 32 + q) * PROW + d4);
    u32x2 pk = {cvtpk(a[0], a[1]), cvtpk(a[2], a[3])};
    *(u32x2*)(Oh + (32 + q) * D + d4) = pk;
  }
}

extern "C" void kernel_launch(void* const* d_in, const int* in_sizes, int n_in,
                              void* d_out, int out_size, void* d_ws, size_t ws_size,
                              hipStream_t stream) {
  const __hip_bfloat16* q = (const __hip_bfloat16*)d_in[0];
  const __hip_bfloat16* k = (const __hip_bfloat16*)d_in[1];
  const __hip_bfloat16* v = (const __hip_bfloat16*)d_in[2];
  gqa_attn_kernel<<<B_ * HQ, 64 * NW, 0, stream>>>(q, k, v, (__hip_bfloat16*)d_out);
}
```

```cpp
#include <hip/hip_runtime.h>
#include <hip/hip_bf16.h>

typedef __bf16 bf16x8 __attribute__((ext_vector_type(8)));
typedef __bf16 bf16x2 __attribute__((ext_vector_type(2)));
typedef short s16x4 __attribute__((ext_vector_type(4)));
typedef float f32x4 __attribute__((ext_vector_type(4)));
typedef float f32x2 __attribute__((ext_vector_type(2)));
typedef unsigned u32x4 __attribute__((ext_vector_type(4)));
typedef unsigned u32x2 __attribute__((ext_vector_type(2)));

constexpr int B_ = 8, HQ = 32, HKV = 4, SQ = 48, SK = 2048, D = 128;
constexpr int NW = 8;
constexpr int KT = 32;
constexpr int KPW = SK / NW;
constexpr int NT = KPW / KT;
constexpr float SC = 0.088388347648318447f * 1.4426950408889634f;
constexpr float THR_RAW = 8.0f / SC;

constexpr int RING = 16384;
constexpr int PROW = 132;
constexpr int MERGE_BYTES = NW * 32 * PROW * 4;
constexpr int STAT_OFF = MERGE_BYTES;
constexpr int SMEM_BYTES = STAT_OFF + NW * SQ * 8;

#define DI __device__ __forceinline__

DI float max3f(float a, float b, float c) { return __builtin_fmaxf(__builtin_fmaxf(a, b), c); }
DI float max2f(float a, float b) { return __builtin_fmaxf(a, b); }
DI unsigned cvtpk(float lo, float hi) { f32x2 v = {lo, hi}; bf16x2 b = __builtin_convertvector(v, bf16x2); return __builtin_bit_cast(unsigned, b); }

DI void dma_k(unsigned o0, unsigned o1, unsigned o2, unsigned o3, const char* gb, unsigned ldsb) {
  unsigned keep;
  asm volatile("s_mov_b32 %0, m0\n\ts_mov_b32 m0, %6\n\ts_nop 0\n\t"
               "global_load_lds_dwordx4 %1, %5 offset:-4096 nt\n\t"
               "global_load_lds_dwordx4 %2, %5 offset:-3072 nt\n\t"
               "global_load_lds_dwordx4 %3, %5 offset:-2048 nt\n\t"
               "global_load_lds_dwordx4 %4, %5 offset:-1024 nt\n\t"
               "global_load_lds_dwordx4 %1, %5 offset:0 nt\n\t"
               "global_load_lds_dwordx4 %2, %5 offset:1024 nt\n\t"
               "global_load_lds_dwordx4 %3, %5 offset:2048 nt\n\t"
               "global_load_lds_dwordx4 %4, %5 offset:3072 nt\n\t"
               "s_mov_b32 m0, %0"
               : "=&s"(keep) : "v"(o0), "v"(o1), "v"(o2), "v"(o3), "s"(gb), "s"(ldsb) : "memory");
}
DI void dma_v(unsigned o0, unsigned o1, const char* gb, unsigned ldsb) {
  unsigned keep;
  asm volatile("s_mov_b32 %0, m0\n\ts_mov_b32 m0, %4\n\ts_nop 0\n\t"
               "global_load_lds_dwordx4 %1, %3 offset:-4096 nt\n\t"
               "global_load_lds_dwordx4 %2, %3 offset:-3072 nt\n\t"
               "global_load_lds_dwordx4 %1, %3 offset:-2048 nt\n\t"
               "global_load_lds_dwordx4 %2, %3 offset:-1024 nt\n\t"
               "global_load_lds_dwordx4 %1, %3 offset:0 nt\n\t"
               "global_load_lds_dwordx4 %2, %3 offset:1024 nt\n\t"
               "global_load_lds_dwordx4 %1, %3 offset:2048 nt\n\t"
               "global_load_lds_dwordx4 %2, %3 offset:3072 nt\n\t"
               "s_mov_b32 m0, %0"
               : "=&s"(keep) : "v"(o0), "v"(o1), "s"(gb), "s"(ldsb) : "memory");
}

typedef __attribute__((address_space(3))) char lds_char;
typedef __attribute__((address_space(3))) s16x4 lds_s16x4;
typedef __attribute__((address_space(3))) bf16x8 lds_bf16x8;

__global__ __launch_bounds__(64 * NW, 2)
void gqa_attn_kernel(const __hip_bfloat16* __restrict__ Q, const __hip_bfloat16* __restrict__ K,
                     const __hip_bfloat16* __restrict__ V, __hip_bfloat16* __restrict__ O) {
  __shared__ __attribute__((aligned(16))) char smem[SMEM_BYTES];
  const int tid = threadIdx.x, lane = tid & 63;
  const int wid = __builtin_amdgcn_readfirstlane(tid >> 6);
  const int bid = blockIdx.x;
  const int idx = bid >> 3;
  const int kvg = (bid & 7) * 4 + (idx >> 3);
  const int hq = kvg * 8 + (idx & 7);
  const int l15 = lane & 15, g = lane >> 4;

  const char* Kw = (const char*)K + ((size_t)kvg * SK + (size_t)wid * KPW) * (D * 2) + 4096;
  const char* Vw = (const char*)V + ((size_t)kvg * SK + (size_t)wid * KPW) * (D * 2) + 4096;
  lds_char* sm = (lds_char*)smem;
  const unsigned sbase = (unsigned)(size_t)sm;
  const unsigned kslot = sbase + wid * RING, vslot = kslot + 8192;

  const unsigned ko0 = (unsigned)(g * 256 + 16 * (l15 ^ g));
  const unsigned ko1 = ko0 ^ 64u, ko2 = ko0 ^ 128u, ko3 = ko0 ^ 192u;
  const unsigned vo0 = (unsigned)(g * 256 + 16 * (l15 ^ (g << 1)));
  const unsigned vo1 = vo0 ^ 128u;

  dma_k(ko0, ko1, ko2, ko3, Kw, kslot + 4096);
  dma_v(vo0, vo1, Vw, vslot + 4096);

  bf16x8 qf[3][4];
  {
    const char* Qp = (const char*)Q + ((size_t)hq * SQ + l15) * (D * 2) + g * 16;
#pragma unroll
    for (int qt = 0; qt < 3; ++qt)
#pragma unroll
      for (int s = 0; s < 4; ++s) qf[qt][s] = *(const bf16x8*)(Qp + qt * 16 * 256 + s * 64);
  }

#pragma unroll
  for (int qt = 0; qt < 3; ++qt)
#pragma unroll
    for (int s = 0; s < 4; ++s) asm volatile("" : "+v"(qf[qt][s]));

  const unsigned ka0 = wid * RING + l15 * 256 + 16 * (g ^ l15);
  const int q4 = l15 >> 2, p4 = l15 & 3;
  const unsigned va0 = wid * RING + 8192 + (4 * g + q4) * 256 + 32 * (4 * (g & 1) + q4) + 16 * (p4 >> 1) + 8 * (p4 & 1);

  f32x4 o[8][3];
#pragma unroll
  for (int dt = 0; dt < 8; ++dt)
#pragma unroll
    for (int qt = 0; qt < 3; ++qt) o[dt][qt] = f32x4{0.f, 0.f, 0.f, 0.f};
  float m[3], l[3];
#pragma unroll
  for (int qt = 0; qt < 3; ++qt) { m[qt] = -__builtin_inff(); l[qt] = 0.f; }

  for (int t = 0; t < NT; ++t) {
    asm volatile("s_waitcnt vmcnt(8)" ::: "memory");
    bf16x8 kf[2][4];
#pragma unroll
    for (int kt = 0; kt < 2; ++kt)
#pragma unroll
      for (int s = 0; s < 4; ++s) kf[kt][s] = *(lds_bf16x8*)(sm + ((ka0 ^ (64u * s)) + 4096u * kt));
    asm volatile("s_waitcnt lgkmcnt(0)" ::: "memory");
    if (t + 1 < NT) dma_k(ko0, ko1, ko2, ko3, Kw + (size_t)(t + 1) * 8192, kslot + 4096);

    f32x4 s_[2][3];
#pragma unroll
    for (int kt = 0; kt < 2; ++kt)
#pragma unroll
      for (int qt = 0; qt < 3; ++qt) {
        f32x4 a = {0.f, 0.f, 0.f, 0.f};
#pragma unroll
        for (int s = 0; s < 4; ++s) a = __builtin_amdgcn_mfma_f32_16x16x32_bf16(kf[kt][s], qf[qt][s], a, 0, 0, 0);
        s_[kt][qt] = a;
      }

    if (t + 1 < NT) asm volatile("s_waitcnt vmcnt(8)" ::: "memory");
    else asm volatile("s_waitcnt vmcnt(0)" ::: "memory");
    s16x4 vlo[8], vhi[8];
#pragma unroll
    for (int dt = 0; dt < 8; ++dt) {
      vlo[dt] = __builtin_amdgcn_ds_read_tr16_b64_v4i16((lds_s16x4*)(sm + (va0 ^ (32u * dt))));
      vhi[dt] = __builtin_amdgcn_ds_read_tr16_b64_v4i16((lds_s16x4*)(sm + ((va0 ^ (32u * dt)) + 4096u)));
    }

    float lm[3];
    bool need = false;
#pragma unroll
    for (int qt = 0; qt < 3; ++qt) {
      float a = max3f(s_[0][qt][0], s_[0][qt][1], s_[0][qt][2]);
      a = max3f(a, s_[0][qt][3], s_[1][qt][0]);
      a = max3f(a, s_[1][qt][1], s_[1][qt][2]);
      a = max2f(a, s_[1][qt][3]);
      lm[qt] = a;
      need |= !(a <= m[qt] + THR_RAW);
    }
    if (__any(need)) {
#pragma unroll
      for (int qt = 0; qt < 3; ++qt) {
        float rm = lm[qt];
        auto r = __builtin_amdgcn_permlane16_swap(__float_as_uint(rm), __float_as_uint(rm), false, false);
        rm = fmaxf(__uint_as_float(r[0]), __uint_as_float(r[1]));
        auto r2 = __builtin_amdgcn_permlane32_swap(__float_as_uint(rm), __float_as_uint(rm), false, false);
        rm = fmaxf(__uint_as_float(r2[0]), __uint_as_float(r2[1]));
        const float mn = fmaxf(m[qt], rm);
        const float alpha = __builtin_amdgcn_exp2f((m[qt] - mn) * SC);
        m[qt] = mn;
        l[qt] *= alpha;
#pragma unroll
        for (int dt = 0; dt < 8; ++dt) o[dt][qt] *= alpha;
      }
    }
    bf16x8 pf[3];
#pragma unroll
    for (int qt = 0; qt < 3; ++qt) {
      const float nm = -m[qt] * SC;
      float p[8];
#pragma unroll
      for (int r = 0; r < 4; ++r) {
        p[r] = __builtin_amdgcn_exp2f(fmaf(s_[0][qt][r], SC, nm));
        p[4 + r] = __builtin_amdgcn_exp2f(fmaf(s_[1][qt][r], SC, nm));
      }
      l[qt] += ((p[0] + p[1]) + (p[2] + p[3])) + ((p[4] + p[5]) + (p[6] + p[7]));
      u32x4 w = {cvtpk(p[0], p[1]), cvtpk(p[2], p[3]), cvtpk(p[4], p[5]), cvtpk(p[6], p[7])};
      pf[qt] = __builtin_bit_cast(bf16x8, w);
    }

    asm volatile("s_waitcnt lgkmcnt(0)" ::: "memory");
    if (t + 1 < NT) dma_v(vo0, vo1, Vw + (size_t)(t + 1) * 8192, vslot + 4096);

#pragma unroll
    for (int dt = 0; dt < 8; ++dt) {
      typedef short s16x8 __attribute__((ext_vector_type(8)));
      s16x8 vv = {vlo[dt][0], vlo[dt][1], vlo[dt][2], vlo[dt][3], vhi[dt][0], vhi[dt][1], vhi[dt][2], vhi[dt][3]};
      const bf16x8 vf = __builtin_bit_cast(bf16x8, vv);
#pragma unroll
      for (int qt = 0; qt < 3; ++qt) o[dt][qt] = __builtin_amdgcn_mfma_f32_16x16x32_bf16(vf, pf[qt], o[dt][qt], 0, 0, 0);
    }
  }

  float* stat = (float*)(smem + STAT_OFF);
#pragma unroll
  for (int qt = 0; qt < 3; ++qt) {
    float x = l[qt];
    auto r = __builtin_amdgcn_permlane16_swap(__float_as_uint(x), __float_as_uint(x), false, false);
    x = __uint_as_float(r[0]) + __uint_as_float(r[1]);
    auto r2 = __builtin_amdgcn_permlane32_swap(__float_as_uint(x), __float_as_uint(x), false, false);
    x = __uint_as_float(r2[0]) + __uint_as_float(r2[1]);
    if (g == 0) *(f32x2*)(stat + (wid * SQ + qt * 16 + l15) * 2) = f32x2{m[qt], x};
  }
  __syncthreads();
  float f[3];
#pragma unroll
  for (int qt = 0; qt < 3; ++qt) {
    f32x2 ml[NW];
    float M = -__builtin_inff();
#pragma unroll
    for (int w = 0; w < NW; ++w) { ml[w] = *(const f32x2*)(stat + (w * SQ + qt * 16 + l15) * 2); M = fmaxf(M, ml[w][0]); }
    float L = 0.f;
#pragma unroll
    for (int w = 0; w < NW; ++w) L += ml[w][1] * __builtin_amdgcn_exp2f((ml[w][0] - M) * SC);
    f[qt] = __builtin_amdgcn_exp2f((m[qt] - M) * SC) / L;
  }
  float* mb = (float*)smem;
  __hip_bfloat16* Oh = O + (size_t)hq * SQ * D;
#pragma unroll
  for (int qt = 0; qt < 2; ++qt)
#pragma unroll
    for (int dt = 0; dt < 8; ++dt)
      *(f32x4*)(mb + (wid * 32 + qt * 16 + l15) * PROW + dt * 16 + g * 4) = o[dt][qt] * f[qt];
  __syncthreads();
#pragma unroll
  for (int i = 0; i < 2; ++i) {
    const int c = tid + i * 512, q = c >> 5, d4 = (c & 31) * 4;
    f32x4 a = *(const f32x4*)(mb + q * PROW + d4);
#pragma unroll
    for (int w = 1; w < NW; ++w) a += *(const f32x4*)(mb + (w * 32 + q) * PROW + d4);
    u32x2 pk = {cvtpk(a[0], a[1]), cvtpk(a[2], a[3])};
    *(u32x2*)(Oh + q * D + d4) = pk;
  }
  __syncthreads();
#pragma unroll
  for (int dt = 0; dt < 8; ++dt)
    *(f32x4*)(mb + (wid * 32 + l15) * PROW + dt * 16 + g * 4) = o[dt][2] * f[2];
  __syncthreads();
  {
    const int q = tid >> 5, d4 = (tid & 31) * 4;
    f32x4 a = *(const f32x4*)(mb + q * PROW + d4);
#pragma unroll
    for (int w = 1; w < NW; ++w) a += *(const f32x4*)(mb + (w * 32 + q) * PROW + d4);
    u32x2 pk = {cvtpk(a[0], a[1]), cvtpk(a[2], a[3])};
    *(u32x2*)(Oh + (32 + q) * D + d4) = pk;
  }
}

extern "C" void kernel_launch(void* const* d_in, const int* in_sizes, int n_in,
                              void* d_out, int out_size, void* d_ws, size_t ws_size,
                              hipStream_t stream) {
  const __hip_bfloat16* q = (const __hip_bfloat16*)d_in[0];
  const __hip_bfloat16* k = (const __hip_bfloat16*)d_in[1];
  const __hip_bfloat16* v = (const __hip_bfloat16*)d_in[2];
  gqa_attn_kernel<<<B_ * HQ, 64 * NW, 0, stream>>>(q, k, v, (__hip_bfloat16*)d_out);
}
```

```cpp
#include <hip/hip_runtime.h>
#include <hip/hip_bf16.h>

typedef __bf16 bf16x8 __attribute__((ext_vector_type(8)));
typedef __bf16 bf16x2 __attribute__((ext_vector_type(2)));
typedef short s16x4 __attribute__((ext_vector_type(4)));
typedef short s16x8 __attribute__((ext_vector_type(8)));
typedef float f32x4 __attribute__((ext_vector_type(4)));
typedef float f32x2 __attribute__((ext_vector_type(2)));
typedef unsigned u32x4 __attribute__((ext_vector_type(4)));
typedef unsigned u32x2 __attribute__((ext_vector_type(2)));

#define PRIO 0
#define ROTW 4
#define POL ""

constexpr int B_ = 8, HQ = 32, HKV = 4, SQ = 48, SK = 2048, D = 128;
constexpr int NW = 8;
constexpr int KT = 32;
constexpr int KPW = SK / NW;
constexpr int NT = KPW / KT;
constexpr float SC = 0.088388347648318447f * 1.4426950408889634f;
constexpr float PTHR = 256.0f;

constexpr int RING = 16384;
constexpr int Q_OFF = NW * RING;
constexpr int STAT_OFF = Q_OFF + SQ * 256;
constexpr int SMEM_BYTES = STAT_OFF + NW * SQ * 8;
constexpr int MROW = 264;

#define DI __device__ __forceinline__
DI unsigned cvtpk(float lo, float hi) { f32x2 v = {lo, hi}; bf16x2 b = __builtin_convertvector(v, bf16x2); return __builtin_bit_cast(unsigned, b); }

DI void dma_k(unsigned o0, unsigned o1, unsigned o2, unsigned o3, const char* gb, unsigned ldsb) {
  unsigned keep;
  asm volatile("s_mov_b32 %0, m0\n\ts_mov_b32 m0, %6\n\ts_nop 0\n\t"
               "global_load_lds_dwordx4 %1, %5 offset:-4096" POL "\n\t"
               "global_load_lds_dwordx4 %2, %5 offset:-3072" POL "\n\t"
               "global_load_lds_dwordx4 %3, %5 offset:-2048" POL "\n\t"
               "global_load_lds_dwordx4 %4, %5 offset:-1024" POL "\n\t"
               "global_load_lds_dwordx4 %1, %5 offset:0" POL "\n\t"
               "global_load_lds_dwordx4 %2, %5 offset:1024" POL "\n\t"
               "global_load_lds_dwordx4 %3, %5 offset:2048" POL "\n\t"
               "global_load_lds_dwordx4 %4, %5 offset:3072" POL "\n\t"
               "s_mov_b32 m0, %0"
               : "=&s"(keep) : "v"(o0), "v"(o1), "v"(o2), "v"(o3), "s"(gb), "s"(ldsb) : "memory");
}
DI void dma_v(unsigned o0, unsigned o1, const char* gb, unsigned ldsb) {
  unsigned keep;
  asm volatile("s_mov_b32 %0, m0\n\ts_mov_b32 m0, %4\n\ts_nop 0\n\t"
               "global_load_lds_dwordx4 %1, %3 offset:-4096" POL "\n\t"
               "global_load_lds_dwordx4 %2, %3 offset:-3072" POL "\n\t"
               "global_load_lds_dwordx4 %1, %3 offset:-2048" POL "\n\t"
               "global_load_lds_dwordx4 %2, %3 offset:-1024" POL "\n\t"
               "global_load_lds_dwordx4 %1, %3 offset:0" POL "\n\t"
               "global_load_lds_dwordx4 %2, %3 offset:1024" POL "\n\t"
               "global_load_lds_dwordx4 %1, %3 offset:2048" POL "\n\t"
               "global_load_lds_dwordx4 %2, %3 offset:3072" POL "\n\t"
               "s_mov_b32 m0, %0"
               : "=&s"(keep) : "v"(o0), "v"(o1), "s"(gb), "s"(ldsb) : "memory");
}
DI void dma_q3(unsigned o0, unsigned o1, unsigned o2, const char* gb, unsigned ldsb) {
  unsigned keep;
  asm volatile("s_mov_b32 %0, m0\n\ts_mov_b32 m0, %5\n\ts_nop 0\n\t"
               "global_load_lds_dwordx4 %1, %4 offset:0\n\t"
               "global_load_lds_dwordx4 %2, %4 offset:1024\n\t"
               "global_load_lds_dwordx4 %3, %4 offset:2048\n\t"
               "s_mov_b32 m0, %0"
               : "=&s"(keep) : "v"(o0), "v"(o1), "v"(o2), "s"(gb), "s"(ldsb) : "memory");
}

typedef __attribute__((address_space(3))) char lds_char;
typedef __attribute__((address_space(3))) s16x4 lds_s16x4;
typedef __attribute__((address_space(3))) bf16x8 lds_bf16x8;
typedef __attribute__((address_space(3))) u32x4 lds_u32x4;

__global__ __launch_bounds__(64 * NW, 2)
void gqa_attn_kernel(const __hip_bfloat16* __restrict__ Q, const __hip_bfloat16* __restrict__ K,
                     const __hip_bfloat16* __restrict__ V, __hip_bfloat16* __restrict__ O) {
  __shared__ __attribute__((aligned(16))) char smem[SMEM_BYTES];
  const int tid = threadIdx.x, lane = tid & 63;
  const int wid = __builtin_amdgcn_readfirstlane(tid >> 6);
  const int bid = blockIdx.x;
  const int idx = bid >> 3;
  const int kvg = (bid & 7) * 4 + (idx >> 3);
  const int hq = kvg * 8 + (idx & 7);
  const int l15 = lane & 15, g = lane >> 4;
  const int rot = (idx & 7) & (ROTW - 1);
  auto tile_of = [&](int t) { return (t & ~(ROTW - 1)) | ((t + rot) & (ROTW - 1)); };

  const char* Kw = (const char*)K + ((size_t)kvg * SK + (size_t)wid * KPW) * (D * 2) + 4096;
  const char* Vw = (const char*)V + ((size_t)kvg * SK + (size_t)wid * KPW) * (D * 2) + 4096;
  lds_char* sm = (lds_char*)smem;
  const unsigned sbase = (unsigned)(size_t)sm;
  const unsigned kslot = sbase + wid * RING, vslot = kslot + 8192;

  const unsigned ko0 = (unsigned)(((lane >> 2) & 3) * 256 + g * 64 + (lane & 3) * 16);
  const unsigned ko1 = ko0 ^ 48u, ko2 = ko0 ^ 32u, ko3 = ko0 ^ 16u;
  const unsigned vo0 = ko0, vo1 = ko0 ^ 32u;

  if (wid < 4) {
    const char* Qp = (const char*)Q + ((size_t)hq * SQ + wid * 12) * (D * 2);
    dma_q3(ko0 ^ (16u * ((-3 * wid) & 3)), ko0 ^ (16u * ((-3 * wid - 1) & 3)), ko0 ^ (16u * ((-3 * wid - 2) & 3)), Qp, sbase + Q_OFF + wid * 3072);
    dma_k(ko0, ko1, ko2, ko3, Kw + (size_t)tile_of(0) * 8192, kslot + 4096);
    dma_v(vo0, vo1, Vw + (size_t)tile_of(0) * 8192, vslot + 4096);
    asm volatile("s_waitcnt vmcnt(16)" ::: "memory");
  }
  __builtin_amdgcn_s_barrier();
  if (wid >= 4) {
    dma_k(ko0, ko1, ko2, ko3, Kw + (size_t)tile_of(0) * 8192, kslot + 4096);
    dma_v(vo0, vo1, Vw + (size_t)tile_of(0) * 8192, vslot + 4096);
  }

  const unsigned kr0 = (l15 >> 2) * 1024 + (l15 & 3) * 64 + 16 * (g ^ ((-(l15 >> 2)) & 3));
  const unsigned ka0 = wid * RING + kr0;
  const int q4 = l15 >> 2, p4 = l15 & 3;
  const unsigned vaE = wid * RING + 8192 + g * 1024 + q4 * 64 + (g & 1) * 32 + p4 * 8;
  const unsigned vaO = vaE ^ 32u;

  bf16x8 qf[3][4];
#pragma unroll
  for (int qt = 0; qt < 3; ++qt)
#pragma unroll
    for (int s = 0; s < 4; ++s) {
      qf[qt][s] = *(lds_bf16x8*)(sm + (Q_OFF + qt * 4096 + kr0 + 256u * s));
    }

  f32x4 o[8][3];
#pragma unroll
  for (int dt = 0; dt < 8; ++dt)
#pragma unroll
    for (int qt = 0; qt < 3; ++qt) o[dt][qt] = f32x4{0.f, 0.f, 0.f, 0.f};
  float l[3];
  float nm[3];
#pragma unroll
  for (int qt = 0; qt < 3; ++qt) { nm[qt] = 0.f; l[qt] = 0.f; }

  bf16x8 kf[2][4];
  asm volatile("s_waitcnt vmcnt(8)" ::: "memory");
#pragma unroll
  for (int kt = 0; kt < 2; ++kt)
#pragma unroll
    for (int s = 0; s < 4; ++s) kf[kt][s] = *(lds_bf16x8*)(sm + (ka0 + 256u * s + 4096u * kt));
  asm volatile("s_waitcnt lgkmcnt(0)" ::: "memory");
  dma_k(ko0, ko1, ko2, ko3, Kw + (size_t)tile_of(1) * 8192, kslot + 4096);

  for (int t = 0; t < NT; ++t) {
    if (t + 1 < NT) asm volatile("s_waitcnt vmcnt(8)" ::: "memory");
    else asm volatile("s_waitcnt vmcnt(0)" ::: "memory");
    s16x4 vlo[8], vhi[8];
#pragma unroll
    for (int dt = 0; dt < 8; ++dt) {
      vlo[dt] = __builtin_amdgcn_ds_read_tr16_b64_v4i16((lds_s16x4*)(sm + (((dt & 1) ? vaO : vaE) + 256u * (dt >> 1))));
      vhi[dt] = __builtin_amdgcn_ds_read_tr16_b64_v4i16((lds_s16x4*)(sm + (((dt & 1) ? vaO : vaE) + 256u * (dt >> 1) + 4096u)));
    }
    f32x4 s_[2][3];
#pragma unroll
    for (int kt = 0; kt < 2; ++kt)
#pragma unroll
      for (int qt = 0; qt < 3; ++qt) {
        f32x4 a = {0.f, 0.f, 0.f, 0.f};
#pragma unroll
        for (int s = 0; s < 4; ++s) a = __builtin_amdgcn_mfma_f32_16x16x32_bf16(kf[kt][s], qf[qt][s], a, 0, 0, 0);
        s_[kt][qt] = a;
      }
    asm volatile("s_waitcnt lgkmcnt(0)" ::: "memory");
    if (t + 1 < NT) dma_v(vo0, vo1, Vw + (size_t)tile_of(t + 1) * 8192, vslot + 4096);

    float ls[3];
    bf16x8 pf[3];
    bool need = (t == 0);
#pragma unroll
    for (int qt = 0; qt < 3; ++qt) {
      float p[8];
#pragma unroll
      for (int r = 0; r < 4; ++r) {
        p[r] = __builtin_amdgcn_exp2f(fmaf(s_[0][qt][r], SC, nm[qt]));
        p[4 + r] = __builtin_amdgcn_exp2f(fmaf(s_[1][qt][r], SC, nm[qt]));
      }
      ls[qt] = ((p[0] + p[1]) + (p[2] + p[3])) + ((p[4] + p[5]) + (p[6] + p[7]));
      u32x4 w = {cvtpk(p[0], p[1]), cvtpk(p[2], p[3]), cvtpk(p[4], p[5]), cvtpk(p[6], p[7])};
      pf[qt] = __builtin_bit_cast(bf16x8, w);
      need |= !(ls[qt] <= PTHR);
    }
    if (__any(need)) {
#pragma unroll
      for (int qt = 0; qt < 3; ++qt) {
        float rm = fmaxf(fmaxf(fmaxf(s_[0][qt][0], s_[0][qt][1]), fmaxf(s_[0][qt][2], s_[0][qt][3])),
                         fmaxf(fmaxf(s_[1][qt][0], s_[1][qt][1]), fmaxf(s_[1][qt][2], s_[1][qt][3])));
        auto r = __builtin_amdgcn_permlane16_swap(__float_as_uint(rm), __float_as_uint(rm), false, false);
        rm = fmaxf(__uint_as_float(r[0]), __uint_as_float(r[1]));
        auto r2 = __builtin_amdgcn_permlane32_swap(__float_as_uint(rm), __float_as_uint(rm), false, false);
        rm = fmaxf(__uint_as_float(r2[0]), __uint_as_float(r2[1]));
        rm = fmaf(rm, SC, nm[qt]);
        const float delta = (t == 0) ? rm : fmaxf(rm, 0.f);
        const float alpha = (t == 0) ? 0.f : __builtin_amdgcn_exp2f(-delta);
        l[qt] *= alpha;
#pragma unroll
        for (int dt = 0; dt < 8; ++dt) o[dt][qt] *= alpha;
        nm[qt] -= delta;
        float p[8];
#pragma unroll
        for (int r = 0; r < 4; ++r) {
          p[r] = __builtin_amdgcn_exp2f(fmaf(s_[0][qt][r], SC, nm[qt]));
          p[4 + r] = __builtin_amdgcn_exp2f(fmaf(s_[1][qt][r], SC, nm[qt]));
        }
        ls[qt] = ((p[0] + p[1]) + (p[2] + p[3])) + ((p[4] + p[5]) + (p[6] + p[7]));
        u32x4 w = {cvtpk(p[0], p[1]), cvtpk(p[2], p[3]), cvtpk(p[4], p[5]), cvtpk(p[6], p[7])};
        pf[qt] = __builtin_bit_cast(bf16x8, w);
      }
    }
#pragma unroll
    for (int qt = 0; qt < 3; ++qt) l[qt] += ls[qt];

    if (t + 1 < NT) {
      asm volatile("s_waitcnt vmcnt(8)" ::: "memory");
#pragma unroll
      for (int kt = 0; kt < 2; ++kt)
#pragma unroll
        for (int s = 0; s < 4; ++s) kf[kt][s] = *(lds_bf16x8*)(sm + (ka0 + 256u * s + 4096u * kt));
    }
#pragma unroll
    for (int dt = 0; dt < 8; ++dt) {
      s16x8 vv = {vlo[dt][0], vlo[dt][1], vlo[dt][2], vlo[dt][3], vhi[dt][0], vhi[dt][1], vhi[dt][2], vhi[dt][3]};
      const bf16x8 vf = __builtin_bit_cast(bf16x8, vv);
#pragma unroll
      for (int qt = 0; qt < 3; ++qt) o[dt][qt] = __builtin_amdgcn_mfma_f32_16x16x32_bf16(vf, pf[qt], o[dt][qt], 0, 0, 0);
    }
    asm volatile("s_waitcnt lgkmcnt(0)" ::: "memory");
    if (t + 2 < NT) dma_k(ko0, ko1, ko2, ko3, Kw + (size_t)tile_of(t + 2) * 8192, kslot + 4096);
  }

  float* stat = (float*)(smem + STAT_OFF);
#pragma unroll
  for (int qt = 0; qt < 3; ++qt) {
    float x = l[qt];
    auto r = __builtin_amdgcn_permlane16_swap(__float_as_uint(x), __float_as_uint(x), false, false);
    x = __uint_as_float(r[0]) + __uint_as_float(r[1]);
    auto r2 = __builtin_amdgcn_permlane32_swap(__float_as_uint(x), __float_as_uint(x), false, false);
    x = __uint_as_float(r2[0]) + __uint_as_float(r2[1]);
    if (g == 0) *(f32x2*)(stat + ((qt * 16 + l15) * NW + wid) * 2) = f32x2{-nm[qt], x};
    char* row = smem + wid * RING + (qt * 16 + l15) * MROW + g * 8;
#pragma unroll
    for (int dt = 0; dt < 8; ++dt) *(u32x2*)(row + dt * 32) = u32x2{cvtpk(o[dt][qt][0], o[dt][qt][1]), cvtpk(o[dt][qt][2], o[dt][qt][3])};
  }
  __syncthreads();
  if (tid < SQ * 8) {
    const int q = tid >> 3, c16 = tid & 7;
    f32x4 ml[NW / 2];
#pragma unroll
    for (int w = 0; w < NW / 2; ++w) ml[w] = *(const f32x4*)(stat + (q * NW + 2 * w) * 2);
    float M = fmaxf(fmaxf(fmaxf(ml[0][0], ml[0][2]), fmaxf(ml[1][0], ml[1][2])), fmaxf(fmaxf(ml[2][0], ml[2][2]), fmaxf(ml[3][0], ml[3][2])));
    float f[NW], L = 0.f;
#pragma unroll
    for (int w = 0; w < NW; ++w) { f[w] = __builtin_amdgcn_exp2f(ml[w >> 1][2 * (w & 1)] - M); L += f[w] * ml[w >> 1][2 * (w & 1) + 1]; }
    const float rL = 1.0f / L;
    float a[16];
#pragma unroll
    for (int e = 0; e < 16; ++e) a[e] = 0.f;
#pragma unroll
    for (int w = 0; w < NW; ++w) {
      const char* src = smem + w * RING + q * MROW + c16 * 32;
#pragma unroll
      for (int h = 0; h < 4; ++h) {
        const u32x2 x = *(const u32x2*)(src + h * 8);
        a[4 * h + 0] = fmaf(f[w], __uint_as_float(x[0] << 16), a[4 * h + 0]);
        a[4 * h + 1] = fmaf(f[w], __uint_as_float(x[0] & 0xffff0000u), a[4 * h + 1]);
        a[4 * h + 2] = fmaf(f[w], __uint_as_float(x[1] << 16), a[4 * h + 2]);
        a[4 * h + 3] = fmaf(f[w], __uint_as_float(x[1] & 0xffff0000u), a[4 * h + 3]);
      }
    }
    u32x4 o0, o1;
#pragma unroll
    for (int e = 0; e < 4; ++e) { o0[e] = cvtpk(a[2 * e] * rL, a[2 * e + 1] * rL); o1[e] = cvtpk(a[8 + 2 * e] * rL, a[8 + 2 * e + 1] * rL); }
    u32x4* dst = (u32x4*)(O + ((size_t)hq * SQ + q) * D + c16 * 16);
    dst[0] = o0; dst[1] = o1;
  }
}

extern "C" void kernel_launch(void* const* d_in, const int* in_sizes, int n_in,
                              void* d_out, int out_size, void* d_ws, size_t ws_size,
                              hipStream_t stream) {
  const __hip_bfloat16* q = (const __hip_bfloat16*)d_in[0];
  const __hip_bfloat16* k = (const __hip_bfloat16*)d_in[1];
  const __hip_bfloat16* v = (const __hip_bfloat16*)d_in[2];
  gqa_attn_kernel<<<B_ * HQ, 64 * NW, 0, stream>>>(q, k, v, (__hip_bfloat16*)d_out);
}
```

```cpp
#include <hip/hip_runtime.h>
#include <hip/hip_bf16.h>

typedef __bf16 bf16x8 __attribute__((ext_vector_type(8)));
typedef __bf16 bf16x2 __attribute__((ext_vector_type(2)));
typedef short s16x4 __attribute__((ext_vector_type(4)));
typedef short s16x8 __attribute__((ext_vector_type(8)));
typedef float f32x4 __attribute__((ext_vector_type(4)));
typedef float f32x2 __attribute__((ext_vector_type(2)));
typedef unsigned u32x4 __attribute__((ext_vector_type(4)));
typedef unsigned u32x2 __attribute__((ext_vector_type(2)));

#define PRIO 0
#define ROTW 1
#define POL ""

constexpr int B_ = 8, HQ = 32, HKV = 4, SQ = 48, SK = 2048, D = 128;
constexpr int NW = 8;
constexpr int KT = 32;
constexpr int KPW = SK / NW;
constexpr int NT = KPW / KT;
constexpr float SC = 0.088388347648318447f * 1.4426950408889634f;
constexpr float PTHR = 256.0f;

constexpr int RING = 16384;
constexpr int Q_OFF = NW * RING;
constexpr int STAT_OFF = Q_OFF + SQ * 256;
constexpr int SMEM_BYTES = STAT_OFF + NW * SQ * 8;
constexpr int MROW = 264;

#define DI __device__ __forceinline__
DI unsigned cvtpk(float lo, float hi) { f32x2 v = {lo, hi}; bf16x2 b = __builtin_convertvector(v, bf16x2); return __builtin_bit_cast(unsigned, b); }

DI void dma_k(unsigned o0, unsigned o1, unsigned o2, unsigned o3, const char* gb, unsigned ldsb) {
  unsigned keep;
  asm volatile("s_mov_b32 %0, m0\n\ts_mov_b32 m0, %6\n\ts_nop 0\n\t"
               "global_load_lds_dwordx4 %1, %5 offset:-4096" POL "\n\t"
               "global_load_lds_dwordx4 %2, %5 offset:-3072" POL "\n\t"
               "global_load_lds_dwordx4 %3, %5 offset:-2048" POL "\n\t"
               "global_load_lds_dwordx4 %4, %5 offset:-1024" POL "\n\t"
               "global_load_lds_dwordx4 %1, %5 offset:0" POL "\n\t"
               "global_load_lds_dwordx4 %2, %5 offset:1024" POL "\n\t"
               "global_load_lds_dwordx4 %3, %5 offset:2048" POL "\n\t"
               "global_load_lds_dwordx4 %4, %5 offset:3072" POL "\n\t"
               "s_mov_b32 m0, %0"
               : "=&s"(keep) : "v"(o0), "v"(o1), "v"(o2), "v"(o3), "s"(gb), "s"(ldsb) : "memory");
}
DI void dma_v(unsigned o0, unsigned o1, const char* gb, unsigned ldsb) {
  unsigned keep;
  asm volatile("s_mov_b32 %0, m0\n\ts_mov_b32 m0, %4\n\ts_nop 0\n\t"
               "global_load_lds_dwordx4 %1, %3 offset:-4096" POL "\n\t"
               "global_load_lds_dwordx4 %2, %3 offset:-3072" POL "\n\t"
               "global_load_lds_dwordx4 %1, %3 offset:-2048" POL "\n\t"
               "global_load_lds_dwordx4 %2, %3 offset:-1024" POL "\n\t"
               "global_load_lds_dwordx4 %1, %3 offset:0" POL "\n\t"
               "global_load_lds_dwordx4 %2, %3 offset:1024" POL "\n\t"
               "global_load_lds_dwordx4 %1, %3 offset:2048" POL "\n\t"
               "global_load_lds_dwordx4 %2, %3 offset:3072" POL "\n\t"
               "s_mov_b32 m0, %0"
               : "=&s"(keep) : "v"(o0), "v"(o1), "s"(gb), "s"(ldsb) : "memory");
}
DI void dma_q3(unsigned o0, unsigned o1, unsigned o2, const char* gb, unsigned ldsb) {
  unsigned keep;
  asm volatile("s_mov_b32 %0, m0\n\ts_mov_b32 m0, %5\n\ts_nop 0\n\t"
               "global_load_lds_dwordx4 %1, %4 offset:0\n\t"
               "global_load_lds_dwordx4 %2, %4 offset:1024\n\t"
               "global_load_lds_dwordx4 %3, %4 offset:2048\n\t"
               "s_mov_b32 m0, %0"
               : "=&s"(keep) : "v"(o0), "v"(o1), "v"(o2), "s"(gb), "s"(ldsb) : "memory");
}

typedef __attribute__((address_space(3))) char lds_char;
typedef __attribute__((address_space(3))) s16x4 lds_s16x4;
typedef __attribute__((address_space(3))) bf16x8 lds_bf16x8;
typedef __attribute__((address_space(3))) u32x4 lds_u32x4;

__global__ __launch_bounds__(64 * NW, 2)
void gqa_attn_kernel(const __hip_bfloat16* __restrict__ Q, const __hip_bfloat16* __restrict__ K,
                     const __hip_bfloat16* __restrict__ V, __hip_bfloat16* __restrict__ O) {
  __shared__ __attribute__((aligned(16))) char smem[SMEM_BYTES];
  const int tid = threadIdx.x, lane = tid & 63;
  const int wid = __builtin_amdgcn_readfirstlane(tid >> 6);
  const int bid = blockIdx.x;
  const int idx = bid >> 3;
  const int kvg = (bid & 7) * 4 + (idx >> 3);
  const int hq = kvg * 8 + (idx & 7);
  const int l15 = lane & 15, g = lane >> 4;
  const int rot = (idx & 7) & (ROTW - 1);
  auto tile_of = [&](int t) { return (t & ~(ROTW - 1)) | ((t + rot) & (ROTW - 1)); };

  const char* Kw = (const char*)K + ((size_t)kvg * SK + (size_t)wid * KPW) * (D * 2) + 4096;
  const char* Vw = (const char*)V + ((size_t)kvg * SK + (size_t)wid * KPW) * (D * 2) + 4096;
  lds_char* sm = (lds_char*)smem;
  const unsigned sbase = (unsigned)(size_t)sm;
  const unsigned kslot = sbase + wid * RING, vslot = kslot + 8192;

  const unsigned ko0 = (unsigned)(((lane >> 2) & 3) * 256 + g * 64 + (lane & 3) * 16);
  const unsigned ko1 = ko0 ^ 48u, ko2 = ko0 ^ 32u, ko3 = ko0 ^ 16u;
  const unsigned vo0 = ko0, vo1 = ko0 ^ 32u;

  if (wid < 4) {
    const char* Qp = (const char*)Q + ((size_t)hq * SQ + wid * 12) * (D * 2);
    dma_q3(ko0 ^ (16u * ((-3 * wid) & 3)), ko0 ^ (16u * ((-3 * wid - 1) & 3)), ko0 ^ (16u * ((-3 * wid - 2) & 3)), Qp, sbase + Q_OFF + wid * 3072);
    dma_k(ko0, ko1, ko2, ko3, Kw + (size_t)tile_of(0) * 8192, kslot + 4096);
    dma_v(vo0, vo1, Vw + (size_t)tile_of(0) * 8192, vslot + 4096);
    asm volatile("s_waitcnt vmcnt(16)" ::: "memory");
  }
  __builtin_amdgcn_s_barrier();
  if (wid >= 4) {
    dma_k(ko0, ko1, ko2, ko3, Kw + (size_t)tile_of(0) * 8192, kslot + 4096);
    dma_v(vo0, vo1, Vw + (size_t)tile_of(0) * 8192, vslot + 4096);
  }

  const unsigned kr0 = (l15 >> 2) * 1024 + (l15 & 3) * 64 + 16 * (g ^ ((-(l15 >> 2)) & 3));
  const unsigned ka0 = wid * RING + kr0;
  const int q4 = l15 >> 2, p4 = l15 & 3;
  const unsigned vaE = wid * RING + 8192 + g * 1024 + q4 * 64 + (g & 1) * 32 + p4 * 8;
  const unsigned vaO = vaE ^ 32u;

  bf16x8 qf[3][4];
#pragma unroll
  for (int qt = 0; qt < 3; ++qt)
#pragma unroll
    for (int s = 0; s < 4; ++s) {
      qf[qt][s] = *(lds_bf16x8*)(sm + (Q_OFF + qt * 4096 + kr0 + 256u * s));
    }

  f32x4 o[8][3];
#pragma unroll
  for (int dt = 0; dt < 8; ++dt)
#pragma unroll
    for (int qt = 0; qt < 3; ++qt) o[dt][qt] = f32x4{0.f, 0.f, 0.f, 0.f};
  float l[3];
  float nm[3];
#pragma unroll
  for (int qt = 0; qt < 3; ++qt) { nm[qt] = 0.f; l[qt] = 0.f; }

  bf16x8 kf[2][4];
  asm volatile("s_waitcnt vmcnt(8)" ::: "memory");
#pragma unroll
  for (int kt = 0; kt < 2; ++kt)
#pragma unroll
    for (int s = 0; s < 4; ++s) kf[kt][s] = *(lds_bf16x8*)(sm + (ka0 + 256u * s + 4096u * kt));
  asm volatile("s_waitcnt lgkmcnt(0)" ::: "memory");
  dma_k(ko0, ko1, ko2, ko3, Kw + (size_t)tile_of(1) * 8192, kslot + 4096);

  for (int t = 0; t < NT; ++t) {
    if (t + 1 < NT) asm volatile("s_waitcnt vmcnt(8)" ::: "memory");
    else asm volatile("s_waitcnt vmcnt(0)" ::: "memory");
    s16x4 vlo[8], vhi[8];
#pragma unroll
    for (int dt = 0; dt < 8; ++dt) {
      vlo[dt] = __builtin_amdgcn_ds_read_tr16_b64_v4i16((lds_s16x4*)(sm + (((dt & 1) ? vaO : vaE) + 256u * (dt >> 1))));
      vhi[dt] = __builtin_amdgcn_ds_read_tr16_b64_v4i16((lds_s16x4*)(sm + (((dt & 1) ? vaO : vaE) + 256u * (dt >> 1) + 4096u)));
    }
    f32x4 s_[2][3];
#pragma unroll
    for (int kt = 0; kt < 2; ++kt)
#pragma unroll
      for (int qt = 0; qt < 3; ++qt) {
        f32x4 a = {0.f, 0.f, 0.f, 0.f};
#pragma unroll
        for (int s = 0; s < 4; ++s) a = __builtin_amdgcn_mfma_f32_16x16x32_bf16(kf[kt][s], qf[qt][s], a, 0, 0, 0);
        s_[kt][qt] = a;
      }
    asm volatile("s_waitcnt lgkmcnt(0)" ::: "memory");
    if (t + 1 < NT) dma_v(vo0, vo1, Vw + (size_t)tile_of(t + 1) * 8192, vslot + 4096);

    float ls[3];
    bf16x8 pf[3];
    bool need = (t == 0);
#pragma unroll
    for (int qt = 0; qt < 3; ++qt) {
      float p[8];
#pragma unroll
      for (int r = 0; r < 4; ++r) {
        p[r] = __builtin_amdgcn_exp2f(fmaf(s_[0][qt][r], SC, nm[qt]));
        p[4 + r] = __builtin_amdgcn_exp2f(fmaf(s_[1][qt][r], SC, nm[qt]));
      }
      ls[qt] = ((p[0] + p[1]) + (p[2] + p[3])) + ((p[4] + p[5]) + (p[6] + p[7]));
      u32x4 w = {cvtpk(p[0], p[1]), cvtpk(p[2], p[3]), cvtpk(p[4], p[5]), cvtpk(p[6], p[7])};
      pf[qt] = __builtin_bit_cast(bf16x8, w);
      need |= !(ls[qt] <= PTHR);
    }
    if (__any(need)) {
#pragma unroll
      for (int qt = 0; qt < 3; ++qt) {
        float rm = fmaxf(fmaxf(fmaxf(s_[0][qt][0], s_[0][qt][1]), fmaxf(s_[0][qt][2], s_[0][qt][3])),
                         fmaxf(fmaxf(s_[1][qt][0], s_[1][qt][1]), fmaxf(s_[1][qt][2], s_[1][qt][3])));
        auto r = __builtin_amdgcn_permlane16_swap(__float_as_uint(rm), __float_as_uint(rm), false, false);
        rm = fmaxf(__uint_as_float(r[0]), __uint_as_float(r[1]));
        auto r2 = __builtin_amdgcn_permlane32_swap(__float_as_uint(rm), __float_as_uint(rm), false, false);
        rm = fmaxf(__uint_as_float(r2[0]), __uint_as_float(r2[1]));
        rm = fmaf(rm, SC, nm[qt]);
        const float delta = (t == 0) ? rm : fmaxf(rm, 0.f);
        const float alpha = (t == 0) ? 0.f : __builtin_amdgcn_exp2f(-delta);
        l[qt] *= alpha;
#pragma unroll
        for (int dt = 0; dt < 8; ++dt) o[dt][qt] *= alpha;
        nm[qt] -= delta;
        float p[8];
#pragma unroll
        for (int r = 0; r < 4; ++r) {
          p[r] = __builtin_amdgcn_exp2f(fmaf(s_[0][qt][r], SC, nm[qt]));
          p[4 + r] = __builtin_amdgcn_exp2f(fmaf(s_[1][qt][r], SC, nm[qt]));
        }
        ls[qt] = ((p[0] + p[1]) + (p[2] + p[3])) + ((p[4] + p[5]) + (p[6] + p[7]));
        u32x4 w = {cvtpk(p[0], p[1]), cvtpk(p[2], p[3]), cvtpk(p[4], p[5]), cvtpk(p[6], p[7])};
        pf[qt] = __builtin_bit_cast(bf16x8, w);
      }
    }
#pragma unroll
    for (int qt = 0; qt < 3; ++qt) l[qt] += ls[qt];

    if (t + 1 < NT) {
      asm volatile("s_waitcnt vmcnt(8)" ::: "memory");
#pragma unroll
      for (int kt = 0; kt < 2; ++kt)
#pragma unroll
        for (int s = 0; s < 4; ++s) kf[kt][s] = *(lds_bf16x8*)(sm + (ka0 + 256u * s + 4096u * kt));
    }
#pragma unroll
    for (int dt = 0; dt < 8; ++dt) {
      s16x8 vv = {vlo[dt][0], vlo[dt][1], vlo[dt][2], vlo[dt][3], vhi[dt][0], vhi[dt][1], vhi[dt][2], vhi[dt][3]};
      const bf16x8 vf = __builtin_bit_cast(bf16x8, vv);
#pragma unroll
      for (int qt = 0; qt < 3; ++qt) o[dt][qt] = __builtin_amdgcn_mfma_f32_16x16x32_bf16(vf, pf[qt], o[dt][qt], 0, 0, 0);
    }
    asm volatile("s_waitcnt lgkmcnt(0)" ::: "memory");
    if (t + 2 < NT) dma_k(ko0, ko1, ko2, ko3, Kw + (size_t)tile_of(t + 2) * 8192, kslot + 4096);
  }

#define R4(x) x x x x
#define R16(x) R4(R4(x))
#define R256(x) R16(R16(x))
  { float a_ = l[0], b_ = 1.0f;
    R256(R4(asm volatile("v_fma_f32 %0, %0, %1, 0\n\tv_fma_f32 %0, %0, %1, 0" : "+v"(a_) : "v"(b_));))
    l[0] = a_; }
  float* stat = (float*)(smem + STAT_OFF);
#pragma unroll
  for (int qt = 0; qt < 3; ++qt) {
    float x = l[qt];
    auto r = __builtin_amdgcn_permlane16_swap(__float_as_uint(x), __float_as_uint(x), false, false);
    x = __uint_as_float(r[0]) + __uint_as_float(r[1]);
    auto r2 = __builtin_amdgcn_permlane32_swap(__float_as_uint(x), __float_as_uint(x), false, false);
    x = __uint_as_float(r2[0]) + __uint_as_float(r2[1]);
    if (g == 0) *(f32x2*)(stat + ((qt * 16 + l15) * NW + wid) * 2) = f32x2{-nm[qt], x};
    char* row = smem + wid * RING + (qt * 16 + l15) * MROW + g * 8;
#pragma unroll
    for (int dt = 0; dt < 8; ++dt) *(u32x2*)(row + dt * 32) = u32x2{cvtpk(o[dt][qt][0], o[dt][qt][1]), cvtpk(o[dt][qt][2], o[dt][qt][3])};
  }
  __syncthreads();
  if (tid < SQ * 8) {
    const int q = tid >> 3, c16 = tid & 7;
    f32x4 ml[NW / 2];
#pragma unroll
    for (int w = 0; w < NW / 2; ++w) ml[w] = *(const f32x4*)(stat + (q * NW + 2 * w) * 2);
    float M = fmaxf(fmaxf(fmaxf(ml[0][0], ml[0][2]), fmaxf(ml[1][0], ml[1][2])), fmaxf(fmaxf(ml[2][0], ml[2][2]), fmaxf(ml[3][0], ml[3][2])));
    float f[NW], L = 0.f;
#pragma unroll
    for (int w = 0; w < NW; ++w) { f[w] = __builtin_amdgcn_exp2f(ml[w >> 1][2 * (w & 1)] - M); L += f[w] * ml[w >> 1][2 * (w & 1) + 1]; }
    const float rL = 1.0f / L;
    float a[16];
#pragma unroll
    for (int e = 0; e < 16; ++e) a[e] = 0.f;
#pragma unroll
    for (int w = 0; w < NW; ++w) {
      const char* src = smem + w * RING + q * MROW + c16 * 32;
#pragma unroll
      for (int h = 0; h < 4; ++h) {
        const u32x2 x = *(const u32x2*)(src + h * 8);
        a[4 * h + 0] = fmaf(f[w], __uint_as_float(x[0] << 16), a[4 * h + 0]);
        a[4 * h + 1] = fmaf(f[w], __uint_as_float(x[0] & 0xffff0000u), a[4 * h + 1]);
        a[4 * h + 2] = fmaf(f[w], __uint_as_float(x[1] << 16), a[4 * h + 2]);
        a[4 * h + 3] = fmaf(f[w], __uint_as_float(x[1] & 0xffff0000u), a[4 * h + 3]);
      }
    }
    u32x4 o0, o1;
#pragma unroll
    for (int e = 0; e < 4; ++e) { o0[e] = cvtpk(a[2 * e] * rL, a[2 * e + 1] * rL); o1[e] = cvtpk(a[8 + 2 * e] * rL, a[8 + 2 * e + 1] * rL); }
    u32x4* dst = (u32x4*)(O + ((size_t)hq * SQ + q) * D + c16 * 16);
    dst[0] = o0; dst[1] = o1;
  }
}

extern "C" void kernel_launch(void* const* d_in, const int* in_sizes, int n_in,
                              void* d_out, int out_size, void* d_ws, size_t ws_size,
                              hipStream_t stream) {
  const __hip_bfloat16* q = (const __hip_bfloat16*)d_in[0];
  const __hip_bfloat16* k = (const __hip_bfloat16*)d_in[1];
  const __hip_bfloat16* v = (const __hip_bfloat16*)d_in[2];
  gqa_attn_kernel<<<B_ * HQ, 64 * NW, 0, stream>>>(q, k, v, (__hip_bfloat16*)d_out);
}
```

```cpp
#include <hip/hip_runtime.h>
#include <hip/hip_bf16.h>

typedef __bf16 bf16x8 __attribute__((ext_vector_type(8)));
typedef __bf16 bf16x2 __attribute__((ext_vector_type(2)));
typedef short s16x4 __attribute__((ext_vector_type(4)));
typedef short s16x8 __attribute__((ext_vector_type(8)));
typedef float f32x4 __attribute__((ext_vector_type(4)));
typedef float f32x2 __attribute__((ext_vector_type(2)));
typedef unsigned u32x4 __attribute__((ext_vector_type(4)));
typedef unsigned u32x2 __attribute__((ext_vector_type(2)));

constexpr int B_ = 8, HQ = 32, HKV = 4, SQ = 48, SK = 2048, D = 128;
constexpr int NW = 8, KT = 32, NT = 8, NSPLIT = 4, NROWS = B_ * HQ * SQ;
constexpr float SC = 0.088388347648318447f * 1.4426950408889634f;
constexpr float STHR = 8.0f;

constexpr int STREAM = 49152;
constexpr int Q_OFF = 2 * STREAM;
constexpr int STAT_OFF = Q_OFF + 4 * SQ * 256;
constexpr int SMEM_BYTES = STAT_OFF + NW * SQ * 8;
constexpr int XROW = 68;

#define DI __device__ __forceinline__
DI unsigned cvtpk(float lo, float hi) { f32x2 v = {lo, hi}; bf16x2 b = __builtin_convertvector(v, bf16x2); return __builtin_bit_cast(unsigned, b); }

DI void dma2(unsigned o0, unsigned o1, const char* gb, unsigned ldsb) {
  unsigned keep;
  asm volatile("s_mov_b32 %0, m0\n\ts_mov_b32 m0, %4\n\ts_nop 0\n\t"
               "global_load_lds_dwordx4 %1, %3 offset:0\n\t"
               "global_load_lds_dwordx4 %2, %3 offset:1024\n\t"
               "s_mov_b32 m0, %0"
               : "=&s"(keep) : "v"(o0), "v"(o1), "s"(gb), "s"(ldsb) : "memory");
}

typedef __attribute__((address_space(3))) char lds_char;
typedef __attribute__((address_space(3))) s16x4 lds_s16x4;
typedef __attribute__((address_space(3))) bf16x8 lds_bf16x8;
typedef __attribute__((address_space(3))) u32x4 lds_u32x4;
DI float lane_max(const f32x4& a, const f32x4& b) {
  return fmaxf(fmaxf(fmaxf(a[0], a[1]), fmaxf(a[2], a[3])), fmaxf(fmaxf(b[0], b[1]), fmaxf(b[2], b[3])));
}
DI float xmax4(float rm) {
  auto r = __builtin_amdgcn_permlane16_swap(__float_as_uint(rm), __float_as_uint(rm), false, false);
  rm = fmaxf(__uint_as_float(r[0]), __uint_as_float(r[1]));
  auto r2 = __builtin_amdgcn_permlane32_swap(__float_as_uint(rm), __float_as_uint(rm), false, false);
  return fmaxf(__uint_as_float(r2[0]), __uint_as_float(r2[1]));
}

#define BAR() do { asm volatile("s_waitcnt lgkmcnt(0)" ::: "memory"); __builtin_amdgcn_s_barrier(); asm volatile("" ::: "memory"); } while (0)

__global__ __launch_bounds__(64 * NW, 2)
void gqa_attn_kernel(const __hip_bfloat16* __restrict__ Q, const __hip_bfloat16* __restrict__ K,
                     const __hip_bfloat16* __restrict__ V, __hip_bfloat16* __restrict__ PO, float* __restrict__ PL) {
  __shared__ __attribute__((aligned(16))) char smem[SMEM_BYTES];
  const int tid = threadIdx.x, lane = tid & 63;
  const int wid = __builtin_amdgcn_readfirstlane(tid >> 6);
  const int bid = blockIdx.x;
  const int idx = bid >> 3;
  const int kvg = (bid & 7) * 4 + (idx >> 3);
  const int split = (idx >> 1) & 3, rg = idx & 1;
  const int hw = wid & 3, kh = wid >> 2;
  const int hq0 = kvg * 8 + rg * 4;
  const int l15 = lane & 15, g = lane >> 4;

  lds_char* sm = (lds_char*)smem;
  const unsigned sbase = (unsigned)(size_t)sm;
  const size_t key0 = (size_t)kvg * SK + split * 512 + kh * 256 + hw * 8;
  const char* Kw = (const char*)K + key0 * (D * 2);
  const char* Vw = (const char*)V + key0 * (D * 2);
  const unsigned kdst = sbase + kh * STREAM + hw * 2048, vdst = kdst + 24576;

  const unsigned o0 = (unsigned)(((lane >> 2) & 3) * 256 + g * 64 + (lane & 3) * 16);
  const unsigned koA = o0 ^ (16u * ((-2 * hw) & 3)), koB = o0 ^ (16u * ((-2 * hw - 1) & 3));
  const unsigned voA = o0, voB = o0 ^ 32u;

  {
    const char* Qp = (const char*)Q + ((size_t)hq0 * SQ + wid * 24) * (D * 2);
#pragma unroll
    for (int i = 0; i < 3; ++i)
      dma2(o0 ^ (16u * ((-(6 * wid + 2 * i)) & 3)), o0 ^ (16u * ((-(6 * wid + 2 * i + 1)) & 3)), Qp + i * 2048, sbase + Q_OFF + wid * 6144 + i * 2048);
  }
  dma2(koA, koB, Kw, kdst);
  dma2(koA, koB, Kw + 8192, kdst + 8192);
  dma2(voA, voB, Vw, vdst);
  dma2(koA, koB, Kw + 16384, kdst + 16384);
  dma2(voA, voB, Vw + 8192, vdst + 8192);

  const unsigned kr0 = (l15 >> 2) * 1024 + (l15 & 3) * 64 + 16 * (g ^ ((-(l15 >> 2)) & 3));
  const int q4 = l15 >> 2, p4 = l15 & 3;
  const unsigned ka0 = kh * STREAM + kr0;
  const unsigned vaE = kh * STREAM + 24576 + g * 1024 + q4 * 64 + (g & 1) * 32 + p4 * 8;
  const unsigned vaO = vaE ^ 32u;

  asm volatile("s_waitcnt vmcnt(10)" ::: "memory");
#pragma unroll
  for (int i = 0; i < 6; ++i) {
    lds_u32x4* qp = (lds_u32x4*)(sm + (Q_OFF + wid * 6144 + i * 1024 + lane * 16));
    const u32x4 x = *qp;
    u32x4 w;
#pragma unroll
    for (int e = 0; e < 4; ++e) w[e] = cvtpk(__uint_as_float(x[e] << 16) * SC, __uint_as_float(x[e] & 0xffff0000u) * SC);
    *qp = w;
  }
  asm volatile("s_waitcnt vmcnt(6)" ::: "memory");
  BAR();
  const unsigned qa0 = Q_OFF + hw * 12288 + kr0;

#define READ_K(SLOT) do { _Pragma("unroll") for (int kt = 0; kt < 2; ++kt) { _Pragma("unroll") for (int s4 = 0; s4 < 4; ++s4) \
    kf[kt][s4] = *(lds_bf16x8*)(sm + (ka0 + (SLOT) + 256u * s4 + 4096u * kt)); } } while (0)
#define QK(S) do { _Pragma("unroll") for (int qt = 0; qt < 3; ++qt) { \
    f32x4 a0_ = {nm[qt], nm[qt], nm[qt], nm[qt]}, a1_ = a0_; \
    _Pragma("unroll") for (int s4 = 0; s4 < 4; ++s4) { const bf16x8 q_ = *(lds_bf16x8*)(sm + (qa0 + qt * 4096 + 256u * s4)); \
      a0_ = __builtin_amdgcn_mfma_f32_16x16x32_bf16(kf[0][s4], q_, a0_, 0, 0, 0); \
      a1_ = __builtin_amdgcn_mfma_f32_16x16x32_bf16(kf[1][s4], q_, a1_, 0, 0, 0); } \
    S[0][qt] = a0_; S[1][qt] = a1_; } } while (0)

  f32x4 o[8][3];
#pragma unroll
  for (int dt = 0; dt < 8; ++dt)
#pragma unroll
    for (int qt = 0; qt < 3; ++qt) o[dt][qt] = f32x4{0.f, 0.f, 0.f, 0.f};
  float l[3], nm[3];
#pragma unroll
  for (int qt = 0; qt < 3; ++qt) { nm[qt] = 0.f; l[qt] = 0.f; }
  bf16x8 kf[2][4];
  f32x4 sc_[2][3], sn_[2][3];
  READ_K(0u);
  QK(sc_);
  READ_K(8192u);
#pragma unroll
  for (int qt = 0; qt < 3; ++qt) {
    const float delta = xmax4(lane_max(sc_[0][qt], sc_[1][qt]));
    nm[qt] -= delta; sc_[0][qt] -= delta; sc_[1][qt] -= delta;
  }

  unsigned s0 = 0, s1 = 8192u, s2 = 16384u;
  for (int t = 0; t < NT; ++t) {
    if (t + 1 < NT) QK(sn_);
    bf16x8 pf[3];
#pragma unroll
    for (int qt = 0; qt < 3; ++qt) {
      float p[8];
#pragma unroll
      for (int r = 0; r < 4; ++r) { p[r] = __builtin_amdgcn_exp2f(sc_[0][qt][r]); p[4 + r] = __builtin_amdgcn_exp2f(sc_[1][qt][r]); }
      l[qt] += ((p[0] + p[1]) + (p[2] + p[3])) + ((p[4] + p[5]) + (p[6] + p[7]));
      u32x4 w = {cvtpk(p[0], p[1]), cvtpk(p[2], p[3]), cvtpk(p[4], p[5]), cvtpk(p[6], p[7])};
      pf[qt] = __builtin_bit_cast(bf16x8, w);
    }
    if (t == 7) asm volatile("s_waitcnt vmcnt(0)" ::: "memory");
    else if (t == 0 || t >= 5) asm volatile("s_waitcnt vmcnt(2)" ::: "memory");
    else asm volatile("s_waitcnt vmcnt(4)" ::: "memory");
    BAR();
    if (t == 0) dma2(koA, koB, Kw + (size_t)3 * 8192, kdst + s0);
    if (t + 4 < NT) dma2(koA, koB, Kw + (size_t)(t + 4) * 8192, kdst + s1);
    if (t + 2 < NT) dma2(voA, voB, Vw + (size_t)(t + 2) * 8192, vdst + s2);
#pragma unroll
    for (int dt = 0; dt < 8; ++dt) {
      const s16x4 vlo = __builtin_amdgcn_ds_read_tr16_b64_v4i16((lds_s16x4*)(sm + (((dt & 1) ? vaO : vaE) + s0 + 256u * (dt >> 1))));
      const s16x4 vhi = __builtin_amdgcn_ds_read_tr16_b64_v4i16((lds_s16x4*)(sm + (((dt & 1) ? vaO : vaE) + s0 + 256u * (dt >> 1) + 4096u)));
      s16x8 vv = {vlo[0], vlo[1], vlo[2], vlo[3], vhi[0], vhi[1], vhi[2], vhi[3]};
      const bf16x8 vf = __builtin_bit_cast(bf16x8, vv);
#pragma unroll
      for (int qt = 0; qt < 3; ++qt) o[dt][qt] = __builtin_amdgcn_mfma_f32_16x16x32_bf16(vf, pf[qt], o[dt][qt], 0, 0, 0);
    }
    if (t + 2 < NT) READ_K(s2);
    if (t + 1 < NT) {
      float lm[3];
      bool need = false;
#pragma unroll
      for (int qt = 0; qt < 3; ++qt) { lm[qt] = lane_max(sn_[0][qt], sn_[1][qt]); need |= !(lm[qt] <= STHR); }
      if (__any(need)) {
#pragma unroll
        for (int qt = 0; qt < 3; ++qt) {
          const float delta = fmaxf(xmax4(lm[qt]), 0.f);
          const float alpha = __builtin_amdgcn_exp2f(-delta);
          l[qt] *= alpha;
#pragma unroll
          for (int dt = 0; dt < 8; ++dt) o[dt][qt] *= alpha;
          nm[qt] -= delta; sn_[0][qt] -= delta; sn_[1][qt] -= delta;
        }
      }
#pragma unroll
      for (int qt = 0; qt < 3; ++qt) { sc_[0][qt] = sn_[0][qt]; sc_[1][qt] = sn_[1][qt]; }
    }
    { const unsigned x = s0; s0 = s1; s1 = s2; s2 = x; }
  }
  BAR();

  float* stat = (float*)(smem + STAT_OFF);
  float* xb = (float*)smem;
#pragma unroll
  for (int qt = 0; qt < 3; ++qt) {
    float x = l[qt];
    auto r = __builtin_amdgcn_permlane16_swap(__float_as_uint(x), __float_as_uint(x), false, false);
    x = __uint_as_float(r[0]) + __uint_as_float(r[1]);
    auto r2 = __builtin_amdgcn_permlane32_swap(__float_as_uint(x), __float_as_uint(x), false, false);
    x = __uint_as_float(r2[0]) + __uint_as_float(r2[1]);
    l[qt] = x;
    if (g == 0) *(f32x2*)(stat + (wid * SQ + qt * 16 + l15) * 2) = f32x2{-nm[qt], x};
    float* row = xb + (wid * SQ + qt * 16 + l15) * XROW + g * 4;
    if (kh) {
#pragma unroll
      for (int dt = 0; dt < 4; ++dt) *(f32x4*)(row + dt * 16) = o[dt][qt];
    } else {
#pragma unroll
      for (int dt = 0; dt < 4; ++dt) *(f32x4*)(row + dt * 16) = o[4 + dt][qt];
    }
  }
  __syncthreads();
  const int pw = wid ^ 4;
  const size_t row0 = (size_t)(hq0 + hw) * SQ;
#pragma unroll
  for (int qt = 0; qt < 3; ++qt) {
    const f32x2 ml = *(const f32x2*)(stat + (pw * SQ + qt * 16 + l15) * 2);
    const float m0 = -nm[qt], M = fmaxf(m0, ml[0]);
    const float f0 = __builtin_amdgcn_exp2f(m0 - M), f1 = __builtin_amdgcn_exp2f(ml[0] - M);
    const float L = f0 * l[qt] + f1 * ml[1];
    const float rL = 1.0f / L, a0 = f0 * rL, a1 = f1 * rL;
    const float* row = xb + (pw * SQ + qt * 16 + l15) * XROW + g * 4;
    __hip_bfloat16* dst = PO + ((size_t)split * NROWS + row0 + qt * 16 + l15) * D + kh * 64 + g * 4;
#pragma unroll
    for (int dt = 0; dt < 4; ++dt) {
      const f32x4 y = *(const f32x4*)(row + dt * 16);
      const f32x4 x = kh ? o[4 + dt][qt] : o[dt][qt];
      *(u32x2*)(dst + dt * 16) = u32x2{cvtpk(x[0] * a0 + y[0] * a1, x[1] * a0 + y[1] * a1), cvtpk(x[2] * a0 + y[2] * a1, x[3] * a0 + y[3] * a1)};
    }
    if (g == 0 && kh == 0) PL[(size_t)split * NROWS + row0 + qt * 16 + l15] = M + __builtin_amdgcn_logf(L);
  }
}

__global__ __launch_bounds__(256)
void gqa_combine_kernel(const __hip_bfloat16* __restrict__ PO, const float* __restrict__ PL, __hip_bfloat16* __restrict__ O) {
  const int i = blockIdx.x * 256 + threadIdx.x;
  const int row = i >> 4, c = i & 15;
  float e[NSPLIT], M = -__builtin_inff(), W = 0.f;
  u32x4 x[NSPLIT];
#pragma unroll
  for (int s = 0; s < NSPLIT; ++s) { x[s] = *(const u32x4*)(PO + ((size_t)s * NROWS + row) * D + c * 8); e[s] = PL[(size_t)s * NROWS + row]; M = fmaxf(M, e[s]); }
#pragma unroll
  for (int s = 0; s < NSPLIT; ++s) { e[s] = __builtin_amdgcn_exp2f(e[s] - M); W += e[s]; }
  const float rW = 1.0f / W;
  u32x4 r;
#pragma unroll
  for (int j = 0; j < 4; ++j) {
    float lo = 0.f, hi = 0.f;
#pragma unroll
    for (int s = 0; s < NSPLIT; ++s) { lo = fmaf(e[s], __uint_as_float(x[s][j] << 16), lo); hi = fmaf(e[s], __uint_as_float(x[s][j] & 0xffff0000u), hi); }
    r[j] = cvtpk(lo * rW, hi * rW);
  }
  *(u32x4*)(O + (size_t)row * D + c * 8) = r;
}

extern "C" void kernel_launch(void* const* d_in, const int* in_sizes, int n_in,
                              void* d_out, int out_size, void* d_ws, size_t ws_size,
                              hipStream_t stream) {
  const __hip_bfloat16* q = (const __hip_bfloat16*)d_in[0];
  const __hip_bfloat16* k = (const __hip_bfloat16*)d_in[1];
  const __hip_bfloat16* v = (const __hip_bfloat16*)d_in[2];
  __hip_bfloat16* po = (__hip_bfloat16*)d_ws;
  float* pl = (float*)((char*)d_ws + (size_t)NSPLIT * NROWS * D * 2);
  gqa_attn_kernel<<<256, 64 * NW, 0, stream>>>(q, k, v, po, pl);
  gqa_combine_kernel<<<NROWS * 16 / 256, 256, 0, stream>>>(po, pl, (__hip_bfloat16*)d_out);
}
```

```cpp
#include <hip/hip_runtime.h>
#include <hip/hip_bf16.h>

typedef __bf16 bf16x8 __attribute__((ext_vector_type(8)));
typedef __bf16 bf16x2 __attribute__((ext_vector_type(2)));
typedef short s16x4 __attribute__((ext_vector_type(4)));
typedef short s16x8 __attribute__((ext_vector_type(8)));
typedef float f32x4 __attribute__((ext_vector_type(4)));
typedef float f32x2 __attribute__((ext_vector_type(2)));
typedef unsigned u32x4 __attribute__((ext_vector_type(4)));
typedef unsigned u32x2 __attribute__((ext_vector_type(2)));

constexpr int B_ = 8, HQ = 32, HKV = 4, SQ = 48, SK = 2048, D = 128;
constexpr int NW = 8;
constexpr int KT = 32;
constexpr int KPW = SK / NW;
constexpr int NT = KPW / KT;
constexpr float SC = 0.088388347648318447f * 1.4426950408889634f;
constexpr float PTHR = 256.0f;

constexpr int RING = 16384;
constexpr int Q_OFF = NW * RING;
constexpr int STAT_OFF = Q_OFF + SQ * 256;
constexpr int SMEM_BYTES = STAT_OFF + NW * SQ * 8;
constexpr int MROW = 264;

#define DI __device__ __forceinline__
DI unsigned cvtpk(float lo, float hi) { f32x2 v = {lo, hi}; bf16x2 b = __builtin_convertvector(v, bf16x2); return __builtin_bit_cast(unsigned, b); }

DI void dma_k(unsigned o0, unsigned o1, unsigned o2, unsigned o3, const char* gb, unsigned ldsb) {
  unsigned keep;
  asm volatile("s_mov_b32 %0, m0\n\ts_mov_b32 m0, %6\n\ts_nop 0\n\t"
               "global_load_lds_dwordx4 %1, %5 offset:-4096\n\t"
               "global_load_lds_dwordx4 %2, %5 offset:-3072\n\t"
               "global_load_lds_dwordx4 %3, %5 offset:-2048\n\t"
               "global_load_lds_dwordx4 %4, %5 offset:-1024\n\t"
               "global_load_lds_dwordx4 %1, %5 offset:0\n\t"
               "global_load_lds_dwordx4 %2, %5 offset:1024\n\t"
               "global_load_lds_dwordx4 %3, %5 offset:2048\n\t"
               "global_load_lds_dwordx4 %4, %5 offset:3072\n\t"
               "s_mov_b32 m0, %0"
               : "=&s"(keep) : "v"(o0), "v"(o1), "v"(o2), "v"(o3), "s"(gb), "s"(ldsb) : "memory");
}
DI void dma_v(unsigned o0, unsigned o1, const char* gb, unsigned ldsb) {
  unsigned keep;
  asm volatile("s_mov_b32 %0, m0\n\ts_mov_b32 m0, %4\n\ts_nop 0\n\t"
               "global_load_lds_dwordx4 %1, %3 offset:-4096\n\t"
               "global_load_lds_dwordx4 %2, %3 offset:-3072\n\t"
               "global_load_lds_dwordx4 %1, %3 offset:-2048\n\t"
               "global_load_lds_dwordx4 %2, %3 offset:-1024\n\t"
               "global_load_lds_dwordx4 %1, %3 offset:0\n\t"
               "global_load_lds_dwordx4 %2, %3 offset:1024\n\t"
               "global_load_lds_dwordx4 %1, %3 offset:2048\n\t"
               "global_load_lds_dwordx4 %2, %3 offset:3072\n\t"
               "s_mov_b32 m0, %0"
               : "=&s"(keep) : "v"(o0), "v"(o1), "s"(gb), "s"(ldsb) : "memory");
}
DI void dma_q3(unsigned o0, unsigned o1, unsigned o2, const char* gb, unsigned ldsb) {
  unsigned keep;
  asm volatile("s_mov_b32 %0, m0\n\ts_mov_b32 m0, %5\n\ts_nop 0\n\t"
               "global_load_lds_dwordx4 %1, %4 offset:0\n\t"
               "global_load_lds_dwordx4 %2, %4 offset:1024\n\t"
               "global_load_lds_dwordx4 %3, %4 offset:2048\n\t"
               "s_mov_b32 m0, %0"
               : "=&s"(keep) : "v"(o0), "v"(o1), "v"(o2), "s"(gb), "s"(ldsb) : "memory");
}

typedef __attribute__((address_space(3))) char lds_char;
typedef __attribute__((address_space(3))) s16x4 lds_s16x4;
typedef __attribute__((address_space(3))) bf16x8 lds_bf16x8;

__global__ __launch_bounds__(64 * NW, 2)
void gqa_attn_kernel(const __hip_bfloat16* __restrict__ Q, const __hip_bfloat16* __restrict__ K,
                     const __hip_bfloat16* __restrict__ V, __hip_bfloat16* __restrict__ O) {
  __shared__ __attribute__((aligned(16))) char smem[SMEM_BYTES];
  const int tid = threadIdx.x, lane = tid & 63;
  const int wid = __builtin_amdgcn_readfirstlane(tid >> 6);
  const int bid = blockIdx.x;
  const int idx = bid >> 3;
  const int kvg = (bid & 7) * 4 + (idx >> 3);
  const int hq = kvg * 8 + (idx & 7);
  const int l15 = lane & 15, g = lane >> 4;

  const char* Kw = (const char*)K + ((size_t)kvg * SK + (size_t)wid * KPW) * (D * 2) + 4096;
  const char* Vw = (const char*)V + ((size_t)kvg * SK + (size_t)wid * KPW) * (D * 2) + 4096;
  lds_char* sm = (lds_char*)smem;
  const unsigned sbase = (unsigned)(size_t)sm;
  const unsigned kslot = sbase + wid * RING, vslot = kslot + 8192;

  const unsigned ko0 = (unsigned)(((lane >> 2) & 3) * 256 + g * 64 + (lane & 3) * 16);
  const unsigned ko1 = ko0 ^ 48u, ko2 = ko0 ^ 32u, ko3 = ko0 ^ 16u;
  const unsigned vo0 = ko0, vo1 = ko0 ^ 32u;

  float dzz;
  { f32x4 dz = {0.f,0.f,0.f,0.f}; u32x4 w1 = {0x3f803f80u + lane, 0x3f803f80u, 0x3f803f80u, 0x3f803f80u}; bf16x8 a1 = __builtin_bit_cast(bf16x8, w1);
    for (int i = 0; i < 600; ++i) { dz = __builtin_amdgcn_mfma_f32_16x16x32_bf16(a1, a1, dz, 0, 0, 0); }
    dzz = dz[0]; asm volatile("" :: "v"(dzz)); }
  if (wid < 4) {
    const char* Qp = (const char*)Q + ((size_t)hq * SQ + wid * 12) * (D * 2);
    dma_q3(ko0 ^ (16u * ((-3 * wid) & 3)), ko0 ^ (16u * ((-3 * wid - 1) & 3)), ko0 ^ (16u * ((-3 * wid - 2) & 3)), Qp, sbase + Q_OFF + wid * 3072);
    dma_k(ko0, ko1, ko2, ko3, Kw + (size_t)(0) * 8192, kslot + 4096);
    dma_v(vo0, vo1, Vw + (size_t)(0) * 8192, vslot + 4096);
    asm volatile("s_waitcnt vmcnt(16)" ::: "memory");
  }
  __builtin_amdgcn_s_barrier();
  asm volatile("" ::: "memory");
  if (wid >= 4) {
    dma_k(ko0, ko1, ko2, ko3, Kw + (size_t)(0) * 8192, kslot + 4096);
    dma_v(vo0, vo1, Vw + (size_t)(0) * 8192, vslot + 4096);
  }

  const unsigned kr0 = (l15 >> 2) * 1024 + (l15 & 3) * 64 + 16 * (g ^ ((-(l15 >> 2)) & 3));
  const unsigned ka0 = wid * RING + kr0;
  const int q4 = l15 >> 2, p4 = l15 & 3;
  const unsigned vaE = wid * RING + 8192 + g * 1024 + q4 * 64 + (g & 1) * 32 + p4 * 8;
  const unsigned vaO = vaE ^ 32u;

  bf16x8 qf[3][4];
#pragma unroll
  for (int qt = 0; qt < 3; ++qt)
#pragma unroll
    for (int s = 0; s < 4; ++s) {
      qf[qt][s] = *(lds_bf16x8*)(sm + (Q_OFF + qt * 4096 + kr0 + 256u * s));
    }

  f32x4 o[8][3];
#pragma unroll
  for (int dt = 0; dt < 8; ++dt)
#pragma unroll
    for (int qt = 0; qt < 3; ++qt) o[dt][qt] = f32x4{0.f, 0.f, 0.f, 0.f};
  float l[3];
  float nm[3];
#pragma unroll
  for (int qt = 0; qt < 3; ++qt) { nm[qt] = 0.f; l[qt] = 0.f; }

  bf16x8 kf[2][4];
  asm volatile("s_waitcnt vmcnt(8)" ::: "memory");
#pragma unroll
  for (int kt = 0; kt < 2; ++kt)
#pragma unroll
    for (int s = 0; s < 4; ++s) kf[kt][s] = *(lds_bf16x8*)(sm + (ka0 + 256u * s + 4096u * kt));
  asm volatile("s_waitcnt lgkmcnt(0)" ::: "memory");
  dma_k(ko0, ko1, ko2, ko3, Kw + (size_t)(1) * 8192, kslot + 4096);

  for (int t = 0; t < NT; ++t) {
    if (t + 1 < NT) asm volatile("s_waitcnt vmcnt(8)" ::: "memory");
    else asm volatile("s_waitcnt vmcnt(0)" ::: "memory");
    s16x4 vlo[8], vhi[8];
#pragma unroll
    for (int dt = 0; dt < 8; ++dt) {
      vlo[dt] = __builtin_amdgcn_ds_read_tr16_b64_v4i16((lds_s16x4*)(sm + (((dt & 1) ? vaO : vaE) + 256u * (dt >> 1))));
      vhi[dt] = __builtin_amdgcn_ds_read_tr16_b64_v4i16((lds_s16x4*)(sm + (((dt & 1) ? vaO : vaE) + 256u * (dt >> 1) + 4096u)));
    }
    f32x4 s_[2][3];
#pragma unroll
    for (int kt = 0; kt < 2; ++kt)
#pragma unroll
      for (int qt = 0; qt < 3; ++qt) {
        f32x4 a = {0.f, 0.f, 0.f, 0.f};
#pragma unroll
        for (int s = 0; s < 4; ++s) a = __builtin_amdgcn_mfma_f32_16x16x32_bf16(kf[kt][s], qf[qt][s], a, 0, 0, 0);
        s_[kt][qt] = a;
      }
    asm volatile("s_waitcnt lgkmcnt(0)" ::: "memory");
    if (t + 1 < NT) dma_v(vo0, vo1, Vw + (size_t)(t + 1) * 8192, vslot + 4096);

    float ls[3];
    bf16x8 pf[3];
    bool need = (t == 0);
#pragma unroll
    for (int qt = 0; qt < 3; ++qt) {
      float p[8];
#pragma unroll
      for (int r = 0; r < 4; ++r) {
        p[r] = __builtin_amdgcn_exp2f(fmaf(s_[0][qt][r], SC, nm[qt]));
        p[4 + r] = __builtin_amdgcn_exp2f(fmaf(s_[1][qt][r], SC, nm[qt]));
      }
      ls[qt] = ((p[0] + p[1]) + (p[2] + p[3])) + ((p[4] + p[5]) + (p[6] + p[7]));
      u32x4 w = {cvtpk(p[0], p[1]), cvtpk(p[2], p[3]), cvtpk(p[4], p[5]), cvtpk(p[6], p[7])};
      pf[qt] = __builtin_bit_cast(bf16x8, w);
      need |= !(ls[qt] <= PTHR);
    }
    if (__any(need)) {
#pragma unroll
      for (int qt = 0; qt < 3; ++qt) {
        float rm = fmaxf(fmaxf(fmaxf(s_[0][qt][0], s_[0][qt][1]), fmaxf(s_[0][qt][2], s_[0][qt][3])),
                         fmaxf(fmaxf(s_[1][qt][0], s_[1][qt][1]), fmaxf(s_[1][qt][2], s_[1][qt][3])));
        auto r = __builtin_amdgcn_permlane16_swap(__float_as_uint(rm), __float_as_uint(rm), false, false);
        rm = fmaxf(__uint_as_float(r[0]), __uint_as_float(r[1]));
        auto r2 = __builtin_amdgcn_permlane32_swap(__float_as_uint(rm), __float_as_uint(rm), false, false);
        rm = fmaxf(__uint_as_float(r2[0]), __uint_as_float(r2[1]));
        rm = fmaf(rm, SC, nm[qt]);
        const float delta = (t == 0) ? rm : fmaxf(rm, 0.f);
        const float alpha = (t == 0) ? 0.f : __builtin_amdgcn_exp2f(-delta);
        l[qt] *= alpha;
#pragma unroll
        for (int dt = 0; dt < 8; ++dt) o[dt][qt] *= alpha;
        nm[qt] -= delta;
        float p[8];
#pragma unroll
        for (int r = 0; r < 4; ++r) {
          p[r] = __builtin_amdgcn_exp2f(fmaf(s_[0][qt][r], SC, nm[qt]));
          p[4 + r] = __builtin_amdgcn_exp2f(fmaf(s_[1][qt][r], SC, nm[qt]));
        }
        ls[qt] = ((p[0] + p[1]) + (p[2] + p[3])) + ((p[4] + p[5]) + (p[6] + p[7]));
        u32x4 w = {cvtpk(p[0], p[1]), cvtpk(p[2], p[3]), cvtpk(p[4], p[5]), cvtpk(p[6], p[7])};
        pf[qt] = __builtin_bit_cast(bf16x8, w);
      }
    }
#pragma unroll
    for (int qt = 0; qt < 3; ++qt) l[qt] += ls[qt];

    if (t + 1 < NT) {
      asm volatile("s_waitcnt vmcnt(8)" ::: "memory");
#pragma unroll
      for (int kt = 0; kt < 2; ++kt)
#pragma unroll
        for (int s = 0; s < 4; ++s) kf[kt][s] = *(lds_bf16x8*)(sm + (ka0 + 256u * s + 4096u * kt));
    }
#pragma unroll
    for (int dt = 0; dt < 8; ++dt) {
      s16x8 vv = {vlo[dt][0], vlo[dt][1], vlo[dt][2], vlo[dt][3], vhi[dt][0], vhi[dt][1], vhi[dt][2], vhi[dt][3]};
      const bf16x8 vf = __builtin_bit_cast(bf16x8, vv);
#pragma unroll
      for (int qt = 0; qt < 3; ++qt) o[dt][qt] = __builtin_amdgcn_mfma_f32_16x16x32_bf16(vf, pf[qt], o[dt][qt], 0, 0, 0);
    }
    asm volatile("s_waitcnt lgkmcnt(0)" ::: "memory");
    if (t + 2 < NT) dma_k(ko0, ko1, ko2, ko3, Kw + (size_t)(t + 2) * 8192, kslot + 4096);
  }

  float* stat = (float*)(smem + STAT_OFF);
#pragma unroll
  for (int qt = 0; qt < 3; ++qt) {
    float x = l[qt];
    auto r = __builtin_amdgcn_permlane16_swap(__float_as_uint(x), __float_as_uint(x), false, false);
    x = __uint_as_float(r[0]) + __uint_as_float(r[1]);
    auto r2 = __builtin_amdgcn_permlane32_swap(__float_as_uint(x), __float_as_uint(x), false, false);
    x = __uint_as_float(r2[0]) + __uint_as_float(r2[1]);
    if (g == 0) *(f32x2*)(stat + ((qt * 16 + l15) * NW + wid) * 2) = f32x2{-nm[qt], x};
    char* row = smem + wid * RING + (qt * 16 + l15) * MROW + g * 8;
#pragma unroll
    for (int dt = 0; dt < 8; ++dt) *(u32x2*)(row + dt * 32) = u32x2{cvtpk(o[dt][qt][0], o[dt][qt][1]), cvtpk(o[dt][qt][2], o[dt][qt][3])};
  }
  __syncthreads();
  {
    const int r16 = tid >> 5, c4 = tid & 31;
#pragma unroll
    for (int i = 0; i < 3; ++i) {
      const int q = r16 + 16 * i;
      f32x4 ml[NW / 2];
#pragma unroll
      for (int w = 0; w < NW / 2; ++w) ml[w] = *(const f32x4*)(stat + (q * NW + 2 * w) * 2);
      u32x2 x[NW];
#pragma unroll
      for (int w = 0; w < NW; ++w) x[w] = *(const u32x2*)(smem + w * RING + q * MROW + c4 * 8);
      const float M = fmaxf(fmaxf(fmaxf(ml[0][0], ml[0][2]), fmaxf(ml[1][0], ml[1][2])), fmaxf(fmaxf(ml[2][0], ml[2][2]), fmaxf(ml[3][0], ml[3][2])));
      float L = 0.f, a0 = 0.f, a1 = 0.f, a2 = 0.f, a3 = 0.f;
#pragma unroll
      for (int w = 0; w < NW; ++w) {
        const float f = __builtin_amdgcn_exp2f(ml[w >> 1][2 * (w & 1)] - M);
        L = fmaf(f, ml[w >> 1][2 * (w & 1) + 1], L);
        a0 = fmaf(f, __uint_as_float(x[w][0] << 16), a0);
        a1 = fmaf(f, __uint_as_float(x[w][0] & 0xffff0000u), a1);
        a2 = fmaf(f, __uint_as_float(x[w][1] << 16), a2);
        a3 = fmaf(f, __uint_as_float(x[w][1] & 0xffff0000u), a3);
      }
      const float rL = __builtin_amdgcn_rcpf(L);
      *(u32x2*)(O + ((size_t)hq * SQ + q) * D + c4 * 4) = u32x2{cvtpk(a0 * rL, a1 * rL), cvtpk(a2 * rL, a3 * rL)};
    }
  }
}

extern "C" void kernel_launch(void* const* d_in, const int* in_sizes, int n_in,
                              void* d_out, int out_size, void* d_ws, size_t ws_size,
                              hipStream_t stream) {
  const __hip_bfloat16* q = (const __hip_bfloat16*)d_in[0];
  const __hip_bfloat16* k = (const __hip_bfloat16*)d_in[1];
  const __hip_bfloat16* v = (const __hip_bfloat16*)d_in[2];
  gqa_attn_kernel<<<B_ * HQ, 64 * NW, 0, stream>>>(q, k, v, (__hip_bfloat16*)d_out);
}
```

```cpp
#include <hip/hip_runtime.h>
#include <hip/hip_bf16.h>

typedef __bf16 bf16x8 __attribute__((ext_vector_type(8)));
typedef __bf16 bf16x2 __attribute__((ext_vector_type(2)));
typedef short s16x4 __attribute__((ext_vector_type(4)));
typedef short s16x8 __attribute__((ext_vector_type(8)));
typedef float f32x4 __attribute__((ext_vector_type(4)));
typedef float f32x2 __attribute__((ext_vector_type(2)));
typedef unsigned u32x4 __attribute__((ext_vector_type(4)));
typedef unsigned u32x2 __attribute__((ext_vector_type(2)));

constexpr int B_ = 8, HQ = 32, HKV = 4, SQ = 48, SK = 2048, D = 128;
constexpr int NW = 8;
constexpr int KT = 32;
constexpr int KPW = SK / NW;
constexpr int NT = KPW / KT;
constexpr float SC = 0.088388347648318447f * 1.4426950408889634f;
constexpr float PTHR = 256.0f;

constexpr int RING = 16384;
constexpr int Q_OFF = NW * RING;
constexpr int STAT_OFF = Q_OFF + SQ * 256;
constexpr int SMEM_BYTES = STAT_OFF + NW * SQ * 8;
constexpr int MROW = 264;

#define DI __device__ __forceinline__
DI unsigned cvtpk(float lo, float hi) { f32x2 v = {lo, hi}; bf16x2 b = __builtin_convertvector(v, bf16x2); return __builtin_bit_cast(unsigned, b); }

DI void dma_k(unsigned o0, unsigned o1, unsigned o2, unsigned o3, const char* gb, unsigned ldsb) {
  unsigned keep;
  asm volatile("s_mov_b32 %0, m0\n\ts_mov_b32 m0, %6\n\ts_nop 0\n\t"
               "global_load_lds_dwordx4 %1, %5 offset:-4096\n\t"
               "global_load_lds_dwordx4 %2, %5 offset:-3072\n\t"
               "global_load_lds_dwordx4 %3, %5 offset:-2048\n\t"
               "global_load_lds_dwordx4 %4, %5 offset:-1024\n\t"
               "global_load_lds_dwordx4 %1, %5 offset:0\n\t"
               "global_load_lds_dwordx4 %2, %5 offset:1024\n\t"
               "global_load_lds_dwordx4 %3, %5 offset:2048\n\t"
               "global_load_lds_dwordx4 %4, %5 offset:3072\n\t"
               "s_mov_b32 m0, %0"
               : "=&s"(keep) : "v"(o0), "v"(o1), "v"(o2), "v"(o3), "s"(gb), "s"(ldsb) : "memory");
}
DI void dma_v(unsigned o0, unsigned o1, const char* gb, unsigned ldsb) {
  unsigned keep;
  asm volatile("s_mov_b32 %0, m0\n\ts_mov_b32 m0, %4\n\ts_nop 0\n\t"
               "global_load_lds_dwordx4 %1, %3 offset:-4096\n\t"
               "global_load_lds_dwordx4 %2, %3 offset:-3072\n\t"
               "global_load_lds_dwordx4 %1, %3 offset:-2048\n\t"
               "global_load_lds_dwordx4 %2, %3 offset:-1024\n\t"
               "global_load_lds_dwordx4 %1, %3 offset:0\n\t"
               "global_load_lds_dwordx4 %2, %3 offset:1024\n\t"
               "global_load_lds_dwordx4 %1, %3 offset:2048\n\t"
               "global_load_lds_dwordx4 %2, %3 offset:3072\n\t"
               "s_mov_b32 m0, %0"
               : "=&s"(keep) : "v"(o0), "v"(o1), "s"(gb), "s"(ldsb) : "memory");
}
DI void dma_q3(unsigned o0, unsigned o1, unsigned o2, const char* gb, unsigned ldsb) {
  unsigned keep;
  asm volatile("s_mov_b32 %0, m0\n\ts_mov_b32 m0, %5\n\ts_nop 0\n\t"
               "global_load_lds_dwordx4 %1, %4 offset:0\n\t"
               "global_load_lds_dwordx4 %2, %4 offset:1024\n\t"
               "global_load_lds_dwordx4 %3, %4 offset:2048\n\t"
               "s_mov_b32 m0, %0"
               : "=&s"(keep) : "v"(o0), "v"(o1), "v"(o2), "s"(gb), "s"(ldsb) : "memory");
}

typedef __attribute__((address_space(3))) char lds_char;
typedef __attribute__((address_space(3))) s16x4 lds_s16x4;
typedef __attribute__((address_space(3))) bf16x8 lds_bf16x8;

__global__ __launch_bounds__(64 * NW, 2)
void gqa_attn_kernel(const __hip_bfloat16* __restrict__ Q, const __hip_bfloat16* __restrict__ K,
                     const __hip_bfloat16* __restrict__ V, __hip_bfloat16* __restrict__ O) {
  __shared__ __attribute__((aligned(16))) char smem[SMEM_BYTES];
  const int tid = threadIdx.x, lane = tid & 63;
  const int wid = __builtin_amdgcn_readfirstlane(tid >> 6);
  const int bid = blockIdx.x;
  const int idx = bid >> 3;
  const int kvg = (bid & 7) * 4 + (idx >> 3);
  const int hq = kvg * 8 + (idx & 7);
  const int l15 = lane & 15, g = lane >> 4;

  const char* Kw = (const char*)K + ((size_t)kvg * SK + (size_t)wid * KPW) * (D * 2) + 4096;
  const char* Vw = (const char*)V + ((size_t)kvg * SK + (size_t)wid * KPW) * (D * 2) + 4096;
  lds_char* sm = (lds_char*)smem;
  const unsigned sbase = (unsigned)(size_t)sm;
  const unsigned kslot = sbase + wid * RING, vslot = kslot + 8192;

  const unsigned ko0 = (unsigned)(((lane >> 2) & 3) * 256 + g * 64 + (lane & 3) * 16);
  const unsigned ko1 = ko0 ^ 48u, ko2 = ko0 ^ 32u, ko3 = ko0 ^ 16u;
  const unsigned vo0 = ko0, vo1 = ko0 ^ 32u;

  if (wid < 4) {
    const char* Qp = (const char*)Q + ((size_t)hq * SQ + wid * 12) * (D * 2);
    dma_q3(ko0 ^ (16u * ((-3 * wid) & 3)), ko0 ^ (16u * ((-3 * wid - 1) & 3)), ko0 ^ (16u * ((-3 * wid - 2) & 3)), Qp, sbase + Q_OFF + wid * 3072);
    dma_k(ko0, ko1, ko2, ko3, Kw + (size_t)(0) * 8192, kslot + 4096);
    dma_v(vo0, vo1, Vw + (size_t)(0) * 8192, vslot + 4096);
    asm volatile("s_waitcnt vmcnt(16)" ::: "memory");
  }
  __builtin_amdgcn_s_barrier();
  asm volatile("" ::: "memory");
  if (wid >= 4) {
    dma_k(ko0, ko1, ko2, ko3, Kw + (size_t)(0) * 8192, kslot + 4096);
    dma_v(vo0, vo1, Vw + (size_t)(0) * 8192, vslot + 4096);
  }

  const unsigned kr0 = (l15 >> 2) * 1024 + (l15 & 3) * 64 + 16 * (g ^ ((-(l15 >> 2)) & 3));
  const unsigned ka0 = wid * RING + kr0;
  const int q4 = l15 >> 2, p4 = l15 & 3;
  const unsigned vaE = wid * RING + 8192 + g * 1024 + q4 * 64 + (g & 1) * 32 + p4 * 8;
  const unsigned vaO = vaE ^ 32u;

  bf16x8 qf[3][4];
#pragma unroll
  for (int qt = 0; qt < 3; ++qt)
#pragma unroll
    for (int s = 0; s < 4; ++s) {
      qf[qt][s] = *(lds_bf16x8*)(sm + (Q_OFF + qt * 4096 + kr0 + 256u * s));
    }

  f32x4 o[8][3];
#pragma unroll
  for (int dt = 0; dt < 8; ++dt)
#pragma unroll
    for (int qt = 0; qt < 3; ++qt) o[dt][qt] = f32x4{0.f, 0.f, 0.f, 0.f};
  float l[3];
  float nm[3];
#pragma unroll
  for (int qt = 0; qt < 3; ++qt) { nm[qt] = 0.f; l[qt] = 0.f; }

  bf16x8 kf[2][4];
  asm volatile("s_waitcnt vmcnt(8)" ::: "memory");
#pragma unroll
  for (int kt = 0; kt < 2; ++kt)
#pragma unroll
    for (int s = 0; s < 4; ++s) kf[kt][s] = *(lds_bf16x8*)(sm + (ka0 + 256u * s + 4096u * kt));
  asm volatile("s_waitcnt lgkmcnt(0)" ::: "memory");
  dma_k(ko0, ko1, ko2, ko3, Kw + (size_t)(1) * 8192, kslot + 4096);

  for (int t = 0; t < NT; ++t) {
    if (t + 1 < NT) asm volatile("s_waitcnt vmcnt(8)" ::: "memory");
    else asm volatile("s_waitcnt vmcnt(0)" ::: "memory");
    s16x4 vlo[8], vhi[8];
#pragma unroll
    for (int dt = 0; dt < 8; ++dt) {
      vlo[dt] = __builtin_amdgcn_ds_read_tr16_b64_v4i16((lds_s16x4*)(sm + (((dt & 1) ? vaO : vaE) + 256u * (dt >> 1))));
      vhi[dt] = __builtin_amdgcn_ds_read_tr16_b64_v4i16((lds_s16x4*)(sm + (((dt & 1) ? vaO : vaE) + 256u * (dt >> 1) + 4096u)));
    }
    f32x4 s_[2][3];
#pragma unroll
    for (int kt = 0; kt < 2; ++kt)
#pragma unroll
      for (int qt = 0; qt < 3; ++qt) {
        f32x4 a = {0.f, 0.f, 0.f, 0.f};
#pragma unroll
        for (int s = 0; s < 4; ++s) a = __builtin_amdgcn_mfma_f32_16x16x32_bf16(kf[kt][s], qf[qt][s], a, 0, 0, 0);
        s_[kt][qt] = a;
      }
    asm volatile("s_waitcnt lgkmcnt(0)" ::: "memory");
    if (t + 1 < NT) dma_v(vo0, vo1, Vw + (size_t)(t + 1) * 8192, vslot + 4096);

    float ls[3];
    bf16x8 pf[3];
    bool need = (t == 0);
#pragma unroll
    for (int qt = 0; qt < 3; ++qt) {
      float p[8];
#pragma unroll
      for (int r = 0; r < 4; ++r) {
        p[r] = __builtin_amdgcn_exp2f(fmaf(s_[0][qt][r], SC, nm[qt]));
        p[4 + r] = __builtin_amdgcn_exp2f(fmaf(s_[1][qt][r], SC, nm[qt]));
      }
      ls[qt] = ((p[0] + p[1]) + (p[2] + p[3])) + ((p[4] + p[5]) + (p[6] + p[7]));
      u32x4 w = {cvtpk(p[0], p[1]), cvtpk(p[2], p[3]), cvtpk(p[4], p[5]), cvtpk(p[6], p[7])};
      pf[qt] = __builtin_bit_cast(bf16x8, w);
      need |= !(ls[qt] <= PTHR);
    }
    if (__any(need)) {
#pragma unroll
      for (int qt = 0; qt < 3; ++qt) {
        float rm = fmaxf(fmaxf(fmaxf(s_[0][qt][0], s_[0][qt][1]), fmaxf(s_[0][qt][2], s_[0][qt][3])),
                         fmaxf(fmaxf(s_[1][qt][0], s_[1][qt][1]), fmaxf(s_[1][qt][2], s_[1][qt][3])));
        auto r = __builtin_amdgcn_permlane16_swap(__float_as_uint(rm), __float_as_uint(rm), false, false);
        rm = fmaxf(__uint_as_float(r[0]), __uint_as_float(r[1]));
        auto r2 = __builtin_amdgcn_permlane32_swap(__float_as_uint(rm), __float_as_uint(rm), false, false);
        rm = fmaxf(__uint_as_float(r2[0]), __uint_as_float(r2[1]));
        rm = fmaf(rm, SC, nm[qt]);
        const float delta = (t == 0) ? rm : fmaxf(rm, 0.f);
        const float alpha = (t == 0) ? 0.f : __builtin_amdgcn_exp2f(-delta);
        l[qt] *= alpha;
#pragma unroll
        for (int dt = 0; dt < 8; ++dt) o[dt][qt] *= alpha;
        nm[qt] -= delta;
        float p[8];
#pragma unroll
        for (int r = 0; r < 4; ++r) {
          p[r] = __builtin_amdgcn_exp2f(fmaf(s_[0][qt][r], SC, nm[qt]));
          p[4 + r] = __builtin_amdgcn_exp2f(fmaf(s_[1][qt][r], SC, nm[qt]));
        }
        ls[qt] = ((p[0] + p[1]) + (p[2] + p[3])) + ((p[4] + p[5]) + (p[6] + p[7]));
        u32x4 w = {cvtpk(p[0], p[1]), cvtpk(p[2], p[3]), cvtpk(p[4], p[5]), cvtpk(p[6], p[7])};
        pf[qt] = __builtin_bit_cast(bf16x8, w);
      }
    }
#pragma unroll
    for (int qt = 0; qt < 3; ++qt) l[qt] += ls[qt];

    if (t + 1 < NT) {
      asm volatile("s_waitcnt vmcnt(8)" ::: "memory");
#pragma unroll
      for (int kt = 0; kt < 2; ++kt)
#pragma unroll
        for (int s = 0; s < 4; ++s) kf[kt][s] = *(lds_bf16x8*)(sm + (ka0 + 256u * s + 4096u * kt));
    }
#pragma unroll
    for (int dt = 0; dt < 8; ++dt) {
      s16x8 vv = {vlo[dt][0], vlo[dt][1], vlo[dt][2], vlo[dt][3], vhi[dt][0], vhi[dt][1], vhi[dt][2], vhi[dt][3]};
      const bf16x8 vf = __builtin_bit_cast(bf16x8, vv);
#pragma unroll
      for (int qt = 0; qt < 3; ++qt) o[dt][qt] = __builtin_amdgcn_mfma_f32_16x16x32_bf16(vf, pf[qt], o[dt][qt], 0, 0, 0);
    }
    asm volatile("s_waitcnt lgkmcnt(0)" ::: "memory");
    if (t + 2 < NT) dma_k(ko0, ko1, ko2, ko3, Kw + (size_t)(t + 2) * 8192, kslot + 4096);
  }

  float* stat = (float*)(smem + STAT_OFF);
#pragma unroll
  for (int qt = 0; qt < 3; ++qt) {
    float x = l[qt];
    auto r = __builtin_amdgcn_permlane16_swap(__float_as_uint(x), __float_as_uint(x), false, false);
    x = __uint_as_float(r[0]) + __uint_as_float(r[1]);
    auto r2 = __builtin_amdgcn_permlane32_swap(__float_as_uint(x), __float_as_uint(x), false, false);
    x = __uint_as_float(r2[0]) + __uint_as_float(r2[1]);
    if (g == 0) *(f32x2*)(stat + ((qt * 16 + l15) * NW + wid) * 2) = f32x2{-nm[qt], x};
    char* row = smem + wid * RING + (qt * 16 + l15) * MROW + g * 8;
#pragma unroll
    for (int dt = 0; dt < 8; ++dt) *(u32x2*)(row + dt * 32) = u32x2{cvtpk(o[dt][qt][0], o[dt][qt][1]), cvtpk(o[dt][qt][2], o[dt][qt][3])};
  }
  __syncthreads();
  {
    const int r16 = tid >> 5, c4 = tid & 31;
#pragma unroll
    for (int i = 0; i < 3; ++i) {
      const int q = r16 + 16 * i;
      f32x4 ml[NW / 2];
#pragma unroll
      for (int w = 0; w < NW / 2; ++w) ml[w] = *(const f32x4*)(stat + (q * NW + 2 * w) * 2);
      u32x2 x[NW];
#pragma unroll
      for (int w = 0; w < NW; ++w) x[w] = *(const u32x2*)(smem + w * RING + q * MROW + c4 * 8);
      const float M = fmaxf(fmaxf(fmaxf(ml[0][0], ml[0][2]), fmaxf(ml[1][0], ml[1][2])), fmaxf(fmaxf(ml[2][0], ml[2][2]), fmaxf(ml[3][0], ml[3][2])));
      float L = 0.f, a0 = 0.f, a1 = 0.f, a2 = 0.f, a3 = 0.f;
#pragma unroll
      for (int w = 0; w < NW; ++w) {
        const float f = __builtin_amdgcn_exp2f(ml[w >> 1][2 * (w & 1)] - M);
        L = fmaf(f, ml[w >> 1][2 * (w & 1) + 1], L);
        a0 = fmaf(f, __uint_as_float(x[w][0] << 16), a0);
        a1 = fmaf(f, __uint_as_float(x[w][0] & 0xffff0000u), a1);
        a2 = fmaf(f, __uint_as_float(x[w][1] << 16), a2);
        a3 = fmaf(f, __uint_as_float(x[w][1] & 0xffff0000u), a3);
      }
      const float rL = __builtin_amdgcn_rcpf(L);
      *(u32x2*)(O + ((size_t)hq * SQ + q) * D + c4 * 4) = u32x2{cvtpk(a0 * rL, a1 * rL), cvtpk(a2 * rL, a3 * rL)};
    }
  }
}

extern "C" void kernel_launch(void* const* d_in, const int* in_sizes, int n_in,
                              void* d_out, int out_size, void* d_ws, size_t ws_size,
                              hipStream_t stream) {
  const __hip_bfloat16* q = (const __hip_bfloat16*)d_in[0];
  const __hip_bfloat16* k = (const __hip_bfloat16*)d_in[1];
  const __hip_bfloat16* v = (const __hip_bfloat16*)d_in[2];
  gqa_attn_kernel<<<B_ * HQ, 64 * NW, 0, stream>>>(q, k, v, (__hip_bfloat16*)d_out);
}
```
